```python
import torch

def _softmax(x, axis=-1):
    return torch.softmax(x, dim=axis)
def _gelu(x):
    return torch.nn.functional.gelu(x, approximate="tanh")
def _silu(x):
    return torch.nn.functional.silu(x)
def _sigmoid(x):
    return torch.sigmoid(x)
def _layernorm(x, axis=-1, eps=1e-5):
    m = x.mean(dim=axis, keepdim=True)
    v = x.var(dim=axis, keepdim=True, unbiased=False)
    return (x - m) / torch.sqrt(v + eps)
def _scatter_add(tgt, idx, src, axis=0):
    return tgt.clone().index_add_(axis, idx, src)
def _segment_sum(src, seg, n):
    out = torch.zeros((n,) + src.shape[1:], dtype=src.dtype)
    return out.index_add_(0, seg, src)
def _pad(x, pw):
    flat = tuple(v for pair in reversed(pw) for v in pair)
    return torch.nn.functional.pad(x, flat)

kernel_name = 'standalone__molecular_angular_gemnet__graph'

def setup_inputs():

    _w = (lambda a: (lambda t: t.to(torch.bfloat16) if t.is_floating_point() else t)(torch.as_tensor(a)))
    N, deg, De, L = 10000, 32, 4, 3
    E = N * deg
    T = E * deg
    NR = 8
    dist = torch.rand(E).to(float)
    angle = (torch.rand(T) * torch.pi).to(float)
    centers = torch.linspace(0.0, 1.0, NR).to(float)
    ang_freq = torch.arange(1, L + 1).to(float)
    return {
        "m": _w((lambda *s: torch.randn(*s, dtype=torch.bfloat16) * 0.05)(E, De)),
        "idx_in": _w((lambda lo, hi, *s: torch.randint(lo, hi, s))(0, E, T).to(torch.int64)),
        "idx_out": _w((lambda lo, hi, *s: torch.randint(lo, hi, s))(0, E, T).to(torch.int64)),
        "dist": _w(dist),
        "angle": _w(angle),
        "centers": _w(centers),
        "ang_freq": _w(ang_freq),
        "w_rbf": _w((lambda *s: torch.randn(*s, dtype=torch.bfloat16) * 0.2)(NR, De)),
        "w_sbf": _w((lambda *s: torch.randn(*s, dtype=torch.bfloat16) * 0.2)(NR * L, De)),
        "w_down": _w((lambda *s: torch.randn(*s, dtype=torch.bfloat16) * 0.2)(De, De)),
        "w_up": _w((lambda *s: torch.randn(*s, dtype=torch.bfloat16) * 0.2)(De, De)),
    }

def reference(m, idx_in, idx_out, dist, angle, centers, ang_freq, w_rbf, w_sbf, w_down, w_up):
    E, De = m.shape[0], m.shape[1]
    NR = centers.shape[0]
    L = ang_freq.shape[0]
    rbf = torch.exp(-10.0 * (dist[:, None] - centers[None, :]) ** 2)
    cbf = torch.cos(ang_freq[None, :] * angle[:, None])
    rbf_t = rbf[idx_out]
    sbf = torch.reshape(rbf_t[:, :, None] * cbf[:, None, :], (-1, NR * L))
    x = (m[idx_in] @ w_down) * (sbf @ w_sbf)
    agg = _segment_sum(x, idx_out, E)
    return m + (agg @ w_up) * (rbf @ w_rbf)
```

```cpp
#include <hip/hip_runtime.h>
#include <hip/hip_bf16.h>
#include <stdint.h>

typedef unsigned short u16;
typedef unsigned int u32;

__device__ __forceinline__ float bf2f(u16 v) { return __uint_as_float((u32)v << 16); }
__device__ __forceinline__ float rbf16(float x) { return (float)(__bf16)x; }
__device__ __forceinline__ u16 f2bf(float x) { __bf16 b = (__bf16)x; return __builtin_bit_cast(u16, b); }

__device__ __forceinline__ void edge_rbf(float d, const u16* centers, float* rbf) {
#pragma unroll
    for (int r = 0; r < 8; ++r) {
        float t = rbf16(d - bf2f(centers[r]));
        t = rbf16(t * t);
        t = rbf16(-10.0f * t);
        rbf[r] = rbf16(__expf(t));
    }
}

__global__ void k_trip(const u16* m, const int* idx_in, const int* idx_out, const u16* dist, const u16* angle,
                       const u16* centers, const u16* freq, const u16* w_sbf, const u16* w_down,
                       float* agg, int T) {
    int t = blockIdx.x * blockDim.x + threadIdx.x;
    if (t >= T) return;
    int ei = idx_in[t], eo = idx_out[t];
    float rbf[8];
    edge_rbf(bf2f(dist[eo]), centers, rbf);
    float a = bf2f(angle[t]);
    float c[3];
#pragma unroll
    for (int l = 0; l < 3; ++l) c[l] = rbf16(cosf(rbf16(bf2f(freq[l]) * a)));
#pragma unroll
    for (int d = 0; d < 4; ++d) {
        float g = 0.f;
#pragma unroll
        for (int r = 0; r < 8; ++r)
#pragma unroll
            for (int l = 0; l < 3; ++l) g += rbf[r] * c[l] * bf2f(w_sbf[(r * 3 + l) * 4 + d]);
        float md = 0.f;
#pragma unroll
        for (int k = 0; k < 4; ++k) md += bf2f(m[ei * 4 + k]) * bf2f(w_down[k * 4 + d]);
        atomicAdd(&agg[eo * 4 + d], md * g);
    }
}

__global__ void k_fin(const u16* m, const u16* dist, const u16* centers, const u16* w_rbf, const u16* w_up,
                      const float* agg, u16* out, int E) {
    int e = blockIdx.x * blockDim.x + threadIdx.x;
    if (e >= E) return;
    float rbf[8];
    edge_rbf(bf2f(dist[e]), centers, rbf);
#pragma unroll
    for (int d = 0; d < 4; ++d) {
        float u = 0.f, g = 0.f;
#pragma unroll
        for (int k = 0; k < 4; ++k) u += rbf16(agg[e * 4 + k]) * bf2f(w_up[k * 4 + d]);
#pragma unroll
        for (int r = 0; r < 8; ++r) g += rbf[r] * bf2f(w_rbf[r * 4 + d]);
        out[e * 4 + d] = f2bf(bf2f(m[e * 4 + d]) + rbf16(rbf16(u) * rbf16(g)));
    }
}

extern "C" void kernel_launch(void* const* d_in, const int* in_sizes, int n_in,
                              void* d_out, int out_size, void* d_ws, size_t ws_size,
                              hipStream_t stream) {
    const u16* m = (const u16*)d_in[0];
    const int* idx_in = (const int*)d_in[1];
    const int* idx_out = (const int*)d_in[2];
    const u16* dist = (const u16*)d_in[3];
    const u16* angle = (const u16*)d_in[4];
    const u16* centers = (const u16*)d_in[5];
    const u16* freq = (const u16*)d_in[6];
    const u16* w_rbf = (const u16*)d_in[7];
    const u16* w_sbf = (const u16*)d_in[8];
    const u16* w_down = (const u16*)d_in[9];
    const u16* w_up = (const u16*)d_in[10];
    int T = in_sizes[1], E = in_sizes[3];
    float* agg = (float*)d_ws;
    hipMemsetAsync(agg, 0, (size_t)E * 16, stream);
    k_trip<<<(T + 255) / 256, 256, 0, stream>>>(m, idx_in, idx_out, dist, angle, centers, freq, w_sbf, w_down, agg, T);
    k_fin<<<(E + 255) / 256, 256, 0, stream>>>(m, dist, centers, w_rbf, w_up, agg, (u16*)d_out, E);
}
```

```cpp
#define DIAG 0
#include <hip/hip_runtime.h>
#include <hip/hip_bf16.h>
#include <stdint.h>

typedef unsigned short u16;
typedef unsigned int u32;
typedef unsigned long long u64;
typedef u32 u32x2 __attribute__((ext_vector_type(2)));
typedef u32 u32x3 __attribute__((ext_vector_type(3)));
typedef u32 u32x4 __attribute__((ext_vector_type(4)));
typedef float f32x4 __attribute__((ext_vector_type(4)));

#define LD_AUX 2
#define ST_AUX 16
#define RL_AUX 0
#define WS_OFF 0

#define STAMP(i) do { if (blockIdx.x == 0 && threadIdx.x == 0) { g_st[2 * (i)] = __builtin_amdgcn_s_memrealtime(); g_st[2 * (i) + 1] = __builtin_amdgcn_s_memtime(); } } while (0)
namespace {

constexpr int kE = 320000;
constexpr int kT = 10240000;
constexpr int NWG = 256;
constexpr int TILE = 20000;
constexpr int TPW = 2;
constexpr int NTILE = NWG * TPW;
constexpr int OSL = kE / NWG;
constexpr int KIN = 4;
constexpr int NB = NWG * KIN;
constexpr int INR = 80640;
constexpr int TILE_BYTES = 126208;
constexpr int OFF_STRIDE = NB + 64;
constexpr int QPT = 5;
constexpr int ACT = 1000;
constexpr u32 MAGIC_O = (u32)((0x100000000ull + OSL - 1) / OSL);
constexpr u32 MAGIC_I = (u32)((0x100000000ull + INR - 1) / INR);
static_assert((u64)(MAGIC_O * (u64)OSL - 0x100000000ull) * kE < 0x100000000ull, "exact division");
static_assert(TILE_BYTES >= (TILE + NB) * 6 && TILE_BYTES % 128 == 0, "tile image size");

__device__ __forceinline__ float bf2f(u16 v) { return __uint_as_float((u32)v << 16); }
__device__ __forceinline__ float rbf16(float x) { return (float)(__bf16)x; }
__device__ __forceinline__ u16 f2bf(float x) { __bf16 b = (__bf16)x; return __builtin_bit_cast(u16, b); }
__device__ __forceinline__ float wf(const u16* p, int i) {
    const u32 v = ((const u32*)p)[i >> 1];
    return __uint_as_float((i & 1) ? (v & 0xffff0000u) : (v << 16));
}
__device__ __forceinline__ u32 mulhi24(u32 a, u32 b) { return (u32)(((u64)(a & 0xffffffu) * (u64)(b & 0xffffffu)) >> 32); }

__device__ __forceinline__ u32 wave_incl_scan(u32 x) {
    x += __builtin_amdgcn_update_dpp(0u, x, 0x111, 0xf, 0xf, false);
    x += __builtin_amdgcn_update_dpp(0u, x, 0x112, 0xf, 0xf, false);
    x += __builtin_amdgcn_update_dpp(0u, x, 0x114, 0xf, 0xf, false);
    x += __builtin_amdgcn_update_dpp(0u, x, 0x118, 0xf, 0xf, false);
    x += __builtin_amdgcn_update_dpp(0u, x, 0x142, 0xa, 0xf, false);
    x += __builtin_amdgcn_update_dpp(0u, x, 0x143, 0xc, 0xf, false);
    return x;
}

__global__ __launch_bounds__(1024) void k_part(const u32* __restrict__ idx_in, const u32* __restrict__ idx_out,
                                               const u16* __restrict__ angle, unsigned char* __restrict__ rec,
                                               u16* __restrict__ offs, unsigned long long* g_st) {
    STAMP(0);
    __shared__ __attribute__((aligned(16))) unsigned char img[TILE_BYTES];
    __shared__ u32 cnt[NB];
    __shared__ u32 cur[NB];
    __shared__ u32 wsum[16];
    const u32 tid = threadIdx.x;
    const bool act = tid < ACT;
    const size_t base = (size_t)blockIdx.x * (TILE * TPW);
    auto r_i = __builtin_amdgcn_make_buffer_rsrc((void*)(idx_in + base), 0, TILE * TPW * 4, 0x00020000);
    auto r_o = __builtin_amdgcn_make_buffer_rsrc((void*)(idx_out + base), 0, TILE * TPW * 4, 0x00020000);
    auto r_a = __builtin_amdgcn_make_buffer_rsrc((void*)(angle + base), 0, TILE * TPW * 2, 0x00020000);
    const u32 o16 = act ? tid * 16 : 0x7ffffff0u;
    const u32 o8 = act ? tid * 8 : 0x7ffffff0u;

    u32x4 vo[QPT], vi[QPT];
    u32x2 va[QPT];
#pragma unroll
    for (int j = 0; j < QPT; ++j) {
        vo[j] = __builtin_amdgcn_raw_buffer_load_b128(r_o, o16, j * ACT * 16, LD_AUX);
        vi[j] = __builtin_amdgcn_raw_buffer_load_b128(r_i, o16, j * ACT * 16, LD_AUX);
        va[j] = __builtin_amdgcn_raw_buffer_load_b64(r_a, o8, j * ACT * 8, LD_AUX);
    }
    cnt[tid] = 0;
    __syncthreads();

#pragma unroll
    for (int tl = 0; tl < TPW; ++tl) {
        u32 key[QPT * 4], ab[QPT * 4];
#pragma unroll
        for (int j = 0; j < QPT; ++j) {
#pragma unroll
            for (int r = 0; r < 4; ++r) {
                const u32 o = vo[j][r], i = vi[j][r];
                const u32 b = mulhi24(o, MAGIC_O);
                const u32 q = (i >= INR) + (i >= 2 * INR) + (i >= 3 * INR);
                const u32 ol = o - b * OSL;
                const u32 ba = (b * KIN + q) * 4;
                const u32 aw = va[j][r >> 1] | 0x80008000u;
                key[j * 4 + r] = (i << 11) | ol;
                ab[j * 4 + r] = ((r & 1) ? (aw & 0xffff0000u) : (aw << 16)) | ba;
                if (act) atomicAdd((u32*)((unsigned char*)cnt + ba), 1u);
                asm volatile("" : "+v"(key[j * 4 + r]), "+v"(ab[j * 4 + r]));
            }
            if (tl + 1 < TPW) {
                vo[j] = __builtin_amdgcn_raw_buffer_load_b128(r_o, o16, ((tl + 1) * QPT + j) * ACT * 16, LD_AUX);
                vi[j] = __builtin_amdgcn_raw_buffer_load_b128(r_i, o16, ((tl + 1) * QPT + j) * ACT * 16, LD_AUX);
                va[j] = __builtin_amdgcn_raw_buffer_load_b64(r_a, o8, ((tl + 1) * QPT + j) * ACT * 8, LD_AUX);
            }
        }
        __syncthreads();
        const u32 c = cnt[tid];
        cnt[tid] = 0;
        const u32 cp = (c + 1) >> 1;
        const u32 inc = wave_incl_scan(cp);
        if ((tid & 63) == 63) wsum[tid >> 6] = inc;
        __syncthreads();
        const u32 ws = wsum[tid & 15];
        const u32 wi = wave_incl_scan((tid & 63) < 16 ? ws : 0u);
        const u32 w = __builtin_amdgcn_readfirstlane(tid >> 6);
        const u32 total = __builtin_amdgcn_readlane(wi, 15);
        const u32 wpre = __builtin_amdgcn_readlane(wi, w) - __builtin_amdgcn_readlane(ws, w);
        const u32 start = wpre + inc - cp;
        cur[tid] = start * 12 + 2;
        if (c & 1) *(u16*)(img + start * 12 + c * 6) = 0;
        const u32 tile = blockIdx.x * TPW + tl;
        offs[tile * OFF_STRIDE + tid] = (u16)start;
        if (tid == 0) offs[tile * OFF_STRIDE + NB] = (u16)total;
        __syncthreads();
        if (act) {
#pragma unroll
            for (int s0 = 0; s0 < QPT * 4; s0 += 10) {
                u32 p[10];
#pragma unroll
                for (int s = 0; s < 10; ++s) p[s] = atomicAdd((u32*)((unsigned char*)cur + (ab[s0 + s] & 0xffffu)), 6u);
#pragma unroll
                for (int s = 0; s < 10; ++s) {
                    *(u32*)(img + (p[s] & ~3u)) = key[s0 + s];
                    *(u16*)(img + (p[s] + 2 * (p[s] & 2u) - 2)) = (u16)(ab[s0 + s] >> 16);
                }
            }
        }
        __syncthreads();
        auto r_w = __builtin_amdgcn_make_buffer_rsrc((void*)(rec + (size_t)tile * TILE_BYTES), 0, (total * 12 + 15) & ~15u, 0x00020000);
#pragma unroll
        for (int j = 0; j < 8; ++j) {
            const u32 o = min(tid * 16 + j * 16384, (u32)(TILE_BYTES - 16));
            const u32x4 v = *(const u32x4*)(img + o);
            __builtin_amdgcn_raw_buffer_store_b128(v, r_w, tid * 16 + j * 16384, 0, ST_AUX);
        }
    }
    STAMP(1);
}

__device__ __forceinline__ void edge_rbf(float d, const u16* centers, float* rbf) {
#pragma unroll
    for (int r = 0; r < 8; ++r) {
        float t = rbf16(d - wf(centers, r));
        t = rbf16(t * t);
        t = rbf16(-10.0f * t);
        rbf[r] = rbf16(__expf(t));
    }
}

__device__ __forceinline__ float cos_rad(float x) {
    return __builtin_amdgcn_cosf(__builtin_amdgcn_fractf(x * 0.15915494309189535f));
}

__global__ __launch_bounds__(1024) void k_acc(const u16* __restrict__ m, const u16* __restrict__ dist,
                                              const u16* __restrict__ centers, const u16* __restrict__ freq,
                                              const u16* __restrict__ w_rbf, const u16* __restrict__ w_sbf,
                                              const u16* __restrict__ w_down, const u16* __restrict__ w_up,
                                              const unsigned char* __restrict__ rec, const u16* __restrict__ offs,
                                              u16* __restrict__ out, unsigned long long* g_st) {
    STAMP(2);
    __shared__ f32x4 tabA[3][OSL];
    __shared__ double acc[4][OSL];
    const u32 tid = threadIdx.x, lane = tid & 63;
    const u32 w = __builtin_amdgcn_readfirstlane(tid >> 6);
    const u32 b = (blockIdx.x & 7) * 32 + (blockIdx.x >> 3);
    const u32 e0 = b * OSL;

    u32 s0 = 0, s1 = 0;
    if (lane < 32) {
        const u16* p = offs + (w * 32 + lane) * OFF_STRIDE + b * KIN;
        s0 = p[0];
        s1 = p[KIN];
    }
    for (u32 e = tid; e < OSL; e += 1024) {
        float rbf[8];
        edge_rbf(bf2f(dist[e0 + e]), centers, rbf);
#pragma unroll
        for (int l = 0; l < 3; ++l) {
            f32x4 a = {0.f, 0.f, 0.f, 0.f};
#pragma unroll
            for (int r = 0; r < 8; ++r)
#pragma unroll
                for (int d = 0; d < 4; ++d) a[d] += rbf[r] * wf(w_sbf, (r * 3 + l) * 4 + d);
            tabA[l][e] = a;
        }
#pragma unroll
        for (int d = 0; d < 4; ++d) acc[d][e] = 0.0;
    }
    float wd[16], fr[3];
#pragma unroll
    for (int i = 0; i < 16; ++i) wd[i] = wf(w_down, i);
#pragma unroll
    for (int l = 0; l < 3; ++l) fr[l] = bf2f(freq[l]);
    __syncthreads();

    auto r_rec = __builtin_amdgcn_make_buffer_rsrc((void*)rec, 0, NTILE * TILE_BYTES, 0x00020000);
    auto r_m = __builtin_amdgcn_make_buffer_rsrc((void*)m, 0, kE * 8, 0x00020000);
    const u32x2* m2 = (const u32x2*)m;
    for (int t = 0; t < 32; ++t) {
        const u32 st = __builtin_amdgcn_readlane(s0, t), en = __builtin_amdgcn_readlane(s1, t);
        const u32 tb = (w * 32 + t) * TILE_BYTES;
        for (u32 p = st + lane; p < en; p += 64) {
            const u32x3 v = __builtin_amdgcn_raw_buffer_load_b96(r_rec, p * 12, tb, RL_AUX);
            const bool v1 = (int)v.y < 0;
            const u32 k1 = v1 ? v.z : v.x;
            const u32x2 g0 = __builtin_amdgcn_raw_buffer_load_b64(r_m, (v.x >> 11) * 8, 0, 0);
            const u32x2 g1 = __builtin_amdgcn_raw_buffer_load_b64(r_m, (k1 >> 11) * 8, 0, 0);
#pragma unroll
            for (int h = 0; h < 2; ++h) {
                const u32 key = h ? k1 : v.x;
                const u32x2 g = h ? g1 : g0;
                const float a = __uint_as_float(h ? (v.y & 0xffff0000u) : (v.y << 16));
                const u32 ol = key & 2047u;
                const float m0 = __uint_as_float(g.x << 16), m1 = __uint_as_float(g.x & 0xffff0000u);
                const float m2f = __uint_as_float(g.y << 16), m3 = __uint_as_float(g.y & 0xffff0000u);
                f32x4 gate = tabA[0][ol] * cos_rad(rbf16(fr[0] * a));
                gate += tabA[1][ol] * cos_rad(rbf16(fr[1] * a));
                gate += tabA[2][ol] * cos_rad(rbf16(fr[2] * a));
                if (h == 0 || v1) {
#pragma unroll
                    for (int d = 0; d < 4; ++d) {
                        const float md = m0 * wd[d] + m1 * wd[4 + d] + m2f * wd[8 + d] + m3 * wd[12 + d];
                        atomicAdd(&acc[d][ol], (double)(md * gate[d]));
                    }
                }
            }
        }
    }
    __syncthreads();
    for (u32 e = tid; e < OSL; e += 1024) {
        float rbf[8];
        edge_rbf(bf2f(dist[e0 + e]), centers, rbf);
        const u32x2 g = m2[e0 + e];
        const float mv[4] = {__uint_as_float(g.x << 16), __uint_as_float(g.x & 0xffff0000u),
                             __uint_as_float(g.y << 16), __uint_as_float(g.y & 0xffff0000u)};
        float ag[4];
#pragma unroll
        for (int k = 0; k < 4; ++k) ag[k] = rbf16((float)acc[k][e]);
        u32 o[4];
#pragma unroll
        for (int d = 0; d < 4; ++d) {
            float u = 0.f, gg = 0.f;
#pragma unroll
            for (int k = 0; k < 4; ++k) u += ag[k] * wf(w_up, k * 4 + d);
#pragma unroll
            for (int r = 0; r < 8; ++r) gg += rbf[r] * wf(w_rbf, r * 4 + d);
            o[d] = f2bf(mv[d] + rbf16(rbf16(u) * rbf16(gg)));
        }
        ((u32x2*)out)[e0 + e] = (u32x2){o[0] | (o[1] << 16), o[2] | (o[3] << 16)};
    }
    STAMP(3);
}

__global__ void k_diag(unsigned long long* g_st) {
    unsigned long long now = __builtin_amdgcn_s_memrealtime();
    unsigned long long q = 0;
    if (DIAG == 1) q = (g_st[4] - g_st[0]) * 10;
    if (DIAG == 2) q = (now - g_st[4]) * 10;
    if (DIAG == 3) q = (g_st[3] - g_st[1]) * 1000 / (g_st[2] - g_st[0]);
    if (DIAG == 4) q = (g_st[7] - g_st[5]) * 1000 / (g_st[6] - g_st[4]);
    if (DIAG == 5) q = (g_st[2] - g_st[0]) * 10;
    if (q > 200000) q = 200000;
    while (__builtin_amdgcn_s_memrealtime() - now < q) __builtin_amdgcn_s_sleep(8);
}
}

extern "C" void kernel_launch(void* const* d_in, const int* in_sizes, int n_in,
                              void* d_out, int out_size, void* d_ws, size_t ws_size,
                              hipStream_t stream) {
    unsigned char* rec = (unsigned char*)d_ws + (size_t)WS_OFF * 1048576;
    u16* offs = (u16*)(rec + (size_t)NTILE * TILE_BYTES);
    k_part<<<NWG, 1024, 0, stream>>>((const u32*)d_in[1], (const u32*)d_in[2], (const u16*)d_in[4], rec, offs, (unsigned long long*)((char*)d_ws + ws_size - 256));
    k_acc<<<NWG, 1024, 0, stream>>>((const u16*)d_in[0], (const u16*)d_in[3], (const u16*)d_in[5], (const u16*)d_in[6],
                                    (const u16*)d_in[7], (const u16*)d_in[8], (const u16*)d_in[9], (const u16*)d_in[10],
                                    rec, offs, (u16*)d_out, (unsigned long long*)((char*)d_ws + ws_size - 256));
    k_diag<<<1, 64, 0, stream>>>((unsigned long long*)((char*)d_ws + ws_size - 256));
}
```

```cpp
#include <hip/hip_runtime.h>
#include <hip/hip_bf16.h>
#include <stdint.h>

typedef unsigned char u8;
typedef unsigned short u16;
typedef unsigned int u32;
typedef unsigned long long u64;
typedef u16 u16x2 __attribute__((ext_vector_type(2)));
typedef u32 u32x2 __attribute__((ext_vector_type(2)));
typedef u32 u32x3 __attribute__((ext_vector_type(3)));
typedef u32 u32x4 __attribute__((ext_vector_type(4)));
typedef short s16x4 __attribute__((ext_vector_type(4)));
typedef __bf16 bf16x2 __attribute__((ext_vector_type(2)));
typedef __bf16 bf16x8 __attribute__((ext_vector_type(8)));
typedef float f32x2 __attribute__((ext_vector_type(2)));
typedef float f32x4 __attribute__((ext_vector_type(4)));
typedef float f32x16 __attribute__((ext_vector_type(16)));
#define LDS __attribute__((address_space(3)))
#define CONST __attribute__((address_space(4)))

namespace {

constexpr int kE = 320000;
constexpr int kT = 10240000;
constexpr int NWG = 256;
constexpr int OSL = kE / NWG;
constexpr int KIN = 4;
constexpr int INR = kE / KIN;
constexpr int INL = INR / 16;
constexpr int NB = NWG * KIN;
constexpr int TPW = 2;
constexpr int NTILE = NWG * TPW;
constexpr int ACT = 1000;
constexpr int QPT = 5;
constexpr u32 MAGIC_O = (u32)((0x100000000ull + OSL - 1) / OSL);
constexpr u32 MAGIC_I = (u32)(0x100000000ull / INR);
static_assert(NTILE * QPT * 4 * ACT == kT && QPT * 4 * ACT + NB < 65536, "tiling");
static_assert((u64)(MAGIC_O * (u64)OSL - 0x100000000ull) * kE < 0x100000000ull, "exact division");
static_assert(((u64)((KIN - 1) * INR + 1) * MAGIC_I >> 32) == KIN - 1, "range q ends at row (q + 1) * INR");

constexpr int IMG_BYTES = ((QPT * 4 * ACT + NB) * 5 + 16 + 127) / 128 * 128;
constexpr int NST = (IMG_BYTES + 16383) / 16384;
constexpr int TAB_BYTES = NB * 2 + 128, TAB_OFF = 64;
constexpr int TILE_BYTES = IMG_BYTES + TAB_BYTES + 128;
constexpr u32 WS_STATS = NTILE * TILE_BYTES, WS_FLAGS = WS_STATS + NWG * 32;
constexpr int AUX_NT = 2, AUX_SC1 = 16;
constexpr u32 OOB = 0x80000000u;
constexpr u32 SPIN_MAX = 1u << 16;
constexpr u32 CODE_BYTES = 38912;

constexpr int EW = (OSL / 2 + 63) / 64;
constexpr int S = 12;
constexpr int NOVF = 4;
constexpr int NSLOT = 16 * S;
constexpr int NBIN = NSLOT - NOVF;
constexpr int GSL = 6;
constexpr u32 BSTR = 520;
constexpr u32 BIN_MUL = (u32)((u64)NBIN * 4 * 16384 / INL);
static_assert(((u64)INL * BIN_MUL >> 16) < NBIN, "bin range");

constexpr u32 L_RBF = 16;
constexpr u32 L_CNT = L_RBF + OSL * 16;
constexpr u32 L_MISC = L_CNT + NSLOT * 4;
constexpr u32 L_FAIL = L_MISC, L_NSUM = L_MISC + 4, L_BAD = L_MISC + 8, L_LOST = L_MISC + 12;
constexpr u32 L_MX = L_MISC + 16, L_SM = L_MISC + 32, L_CS = L_MISC + 48, L_BUMP = L_MISC + 64;
constexpr u32 L_CST = L_MISC + 80;
constexpr u32 L_W4 = L_CST + 1024;
constexpr u32 L_SNAP = L_W4 + 128;
constexpr u32 L_PH = L_SNAP + 64;
constexpr u32 L_HIST = L_PH;
constexpr u32 L_CUR = L_HIST + NB * 4;
constexpr u32 L_WSUM = L_CUR + NB * 4;
constexpr u32 L_WSTAT = L_WSUM + 64;
constexpr u32 L_TAB = L_WSTAT + 512;
constexpr u32 L_STAT = L_TAB + 2 * TAB_BYTES;
constexpr u32 L_IMG = L_STAT + 32;
constexpr u32 L_GO = L_IMG + IMG_BYTES;
constexpr u32 L_ACC0 = L_PH;
constexpr u32 L_ACC1 = L_ACC0 + OSL * 8;
constexpr u32 SCR_B = 208, SCR_W = KIN * SCR_B;
constexpr u32 L_SCR = L_ACC1 + OSL * 8;
constexpr u32 L_BINS = L_SCR + 16 * SCR_W;
constexpr u32 L_TOTAL = L_BINS + NSLOT * BSTR;
static_assert(L_TOTAL <= 163840 && L_TOTAL >= L_GO + 256 && L_GO >= L_BINS && L_TOTAL >= OSL * 80 + 16 * 1280 &&
              L_MISC % 16 == 0 && L_PH % 16 == 0 && L_IMG % 16 == 0 && L_BINS % 8 == 0, "LDS map");

constexpr int VBITS = 20;
constexpr int NCAP = 110;
constexpr int EXP0 = 4, EXP1 = 64;
constexpr u32 BM0 = ((u32)EXP0 << 23) | 0x400000u;
constexpr u32 BM1 = ((u32)EXP1 << 23) | 0x400000u;
constexpr int SH0 = (EXP0 - 150 + VBITS) / 2, SH1 = (EXP1 - 150 + VBITS) / 2;
static_assert(SH0 * 2 == EXP0 - 150 + VBITS && SH1 * 2 == EXP1 - 150 + VBITS, "even split");
static_assert((u64)NCAP * (BM0 + (1u << VBITS)) < (1ull << 32) && (u64)NCAP << (VBITS + 1) < BM1 &&
              (((u64)NCAP << VBITS) * 2 + BM0) / (2 * BM0) * BM1 + ((u64)NCAP << VBITS) < (1ull << 31), "count decode");
static_assert(S == 12 && NOVF == 4 && BIN_MUL == 2464 && L_ACC0 == 22080 && L_ACC1 == 32080 && L_CNT == 20016 && SCR_B == 208 &&
              L_CST == 20864 && L_MX == 20800 && L_W4 == 21888 && SH0 == -63 && SH1 == -33 && OSL == 1250 &&
              L_BINS == 55392 && BM0 == 37748736 && BM1 == 541065216 && INR == 80000, "constants baked into the main loop");

struct Params {
    const u16* m;
    const u32 *idx_in, *idx_out;
    const u16 *dist, *angle, *centers, *freq, *w_rbf, *w_sbf, *w_down, *w_up;
    u8* rec;
    u32* flags;
    u16* out;
};

__device__ __forceinline__ const CONST Params* fresh_params() {
    auto p = (const CONST Params*)__builtin_amdgcn_kernarg_segment_ptr();
    asm volatile("" : "+s"(p));
    return p;
}

__device__ __forceinline__ float bf2f(u16 v) { return __uint_as_float((u32)v << 16); }
__device__ __forceinline__ float lo2f(u32 v) { return __uint_as_float(v << 16); }
__device__ __forceinline__ float hi2f(u32 v) { return __uint_as_float(v & 0xffff0000u); }
__device__ __forceinline__ u32 pk_bf16(float lo, float hi) {
    return __builtin_bit_cast(u32, __builtin_convertvector((f32x2){lo, hi}, bf16x2));
}
__device__ __forceinline__ float rbf16(float x) { return __uint_as_float(pk_bf16(0.f, x)); }
__device__ __forceinline__ float wf(const u16* p, int i) {
    const u32 v = ((const CONST u32*)(size_t)p)[i >> 1];
    return (i & 1) ? hi2f(v) : lo2f(v);
}
__device__ __forceinline__ u32 mulhi24(u32 a, u32 b) { return (u32)(((u64)(a & 0xffffffu) * (u64)(b & 0xffffffu)) >> 32); }
__device__ __forceinline__ auto raw_rsrc(const void* p, u32 bytes) {
    return __builtin_amdgcn_make_buffer_rsrc((void*)p, 0, bytes, 0x00020000);
}

template <typename T>
__device__ __forceinline__ LDS T* lp(u32 a) { return (LDS T*)(size_t)a; }
__device__ __forceinline__ u32 lds_add(u32 a, u32 v) {
    return __hip_atomic_fetch_add(lp<u32>(a), v, __ATOMIC_RELAXED, __HIP_MEMORY_SCOPE_WORKGROUP);
}

template <int CTRL, int RM>
__device__ __forceinline__ u32 dpp0(u32 x) { return __builtin_amdgcn_update_dpp(0u, x, CTRL, RM, 0xf, false); }
template <int CTRL, int RM>
__device__ __forceinline__ float dpp0f(float x) { return __uint_as_float(dpp0<CTRL, RM>(__float_as_uint(x))); }

__device__ __forceinline__ u32 half_incl_scan(u32 x) {
    x += dpp0<0x111, 0xf>(x);
    x += dpp0<0x112, 0xf>(x);
    x += dpp0<0x114, 0xf>(x);
    x += dpp0<0x118, 0xf>(x);
    x += dpp0<0x142, 0xa>(x);
    return x;
}
__device__ __forceinline__ u32 wave_incl_scan(u32 x) {
    x = half_incl_scan(x);
    return x + dpp0<0x143, 0xc>(x);
}
__device__ __forceinline__ u32 wave_max_u32(u32 x) {
    x = max(x, dpp0<0x111, 0xf>(x));
    x = max(x, dpp0<0x112, 0xf>(x));
    x = max(x, dpp0<0x114, 0xf>(x));
    x = max(x, dpp0<0x118, 0xf>(x));
    x = max(x, dpp0<0x142, 0xa>(x));
    x = max(x, dpp0<0x143, 0xc>(x));
    return __builtin_amdgcn_readlane(x, 63);
}
__device__ __forceinline__ float wave_sum_f32(float x) {
    x += dpp0f<0x111, 0xf>(x);
    x += dpp0f<0x112, 0xf>(x);
    x += dpp0f<0x114, 0xf>(x);
    x += dpp0f<0x118, 0xf>(x);
    x += dpp0f<0x142, 0xa>(x);
    x += dpp0f<0x143, 0xc>(x);
    return __uint_as_float(__builtin_amdgcn_readlane(__float_as_uint(x), 63));
}

__device__ __forceinline__ f32x16 mfma32(u32x4 a, u32x4 b) {
    const f32x16 z = {0.f, 0.f, 0.f, 0.f, 0.f, 0.f, 0.f, 0.f, 0.f, 0.f, 0.f, 0.f, 0.f, 0.f, 0.f, 0.f};
    return __builtin_amdgcn_mfma_f32_32x32x16_bf16(__builtin_bit_cast(bf16x8, a), __builtin_bit_cast(bf16x8, b), z, 0, 0, 0);
}
__device__ __forceinline__ f32x4 mfma4(u32x2 a, u32x2 b) {
    const f32x4 z = {0.f, 0.f, 0.f, 0.f};
    return __builtin_amdgcn_mfma_f32_4x4x4bf16_1k(__builtin_bit_cast(s16x4, a), __builtin_bit_cast(s16x4, b), z, 0, 0, 0);
}

__device__ __forceinline__ float radial(float d, float center) {
    float t = rbf16(d - center);
    t = rbf16(t * t);
    t = rbf16(-10.0f * t);
    return rbf16(__expf(t));
}

__device__ __forceinline__ u32 scale_w(u32 wbits, int sh, bool& ok) {
    const int ex = (int)((wbits >> 7) & 0xffu);
    const bool zero = (wbits & 0x7fffu) == 0;
    const int ne = ex + sh;
    ok &= zero | ((ex > 0) & (ex < 255) & (ne > 0) & (ne < 255));
    return zero ? 0u : ((wbits + ((u32)sh << 7)) & 0xffffu);
}

__device__ __forceinline__ int angle_base(float f0, float f1, float f2) {
    const u32 fm = max(__float_as_uint(fabsf(f0)), max(__float_as_uint(fabsf(f1)), __float_as_uint(fabsf(f2))));
    return min(243 - (int)((fm + 0x7fffffu) >> 23), 239);
}

__device__ __forceinline__ void column4(const u16* wm, u32 lane, u32 (&c)[4]) {
#pragma unroll
    for (int k = 0; k < 4; ++k) {
        const u64 row = ((const CONST u64*)(size_t)wm)[k];
        c[k] = (u32)(row >> (16 * (lane & 3))) & 0xffffu;
    }
}

struct EdgeIn { u32 wdw, dd; };

__device__ __forceinline__ EdgeIn edge_tables_load(const Params& P, u32 e0, u32 tid) {
    const u32 lane = tid & 63;
    EdgeIn in;
    in.wdw = *(lane < 48 ? (const u32*)P.w_sbf + lane : (const u32*)P.w_rbf + (lane - 48));
    in.dd = ((const u32*)(P.dist + e0))[min(tid, (u32)(OSL / 2 - 1))];
    return in;
}

__device__ __forceinline__ u32x4 edge_tables(const Params& P, const EdgeIn& in, u32 tid, u32 w) {
    const u32 lane = tid & 63;
    const u32 row = lane & 31, f = (row & 3) + 4 * (row >> 3);
    const bool own = ((row >> 2) & 1) == (lane >> 5);
    const u32 src = f < 12 ? f >> 1 : 48 + ((f - 12) >> 1), step = f < 12 ? 6 : 2;
    bool ok = true;
    u32 wr[8];
#pragma unroll
    for (int j = 0; j < 8; ++j) {
        const u32 v = __builtin_amdgcn_ds_bpermute((src + j * step) * 4, in.wdw);
        wr[j] = own ? ((f & 1) ? v >> 16 : v & 0xffffu) : 0u;
    }
    const u32x4 wa = {wr[0] | wr[1] << 16, wr[2] | wr[3] << 16, wr[4] | wr[5] << 16, wr[6] | wr[7] << 16};
    if (w == 2) {
        float cs = 0.f;
#pragma unroll
        for (int j = 0; j < 8; ++j) {
            cs += fabsf(lo2f(wr[j]));
            wr[j] = scale_w(f >= 12 ? 0u : wr[j], (f & 3) == 0 ? SH0 : SH1, ok);
        }
        ok &= cs < 1e12f;
        if (own && f < 12) __hip_atomic_fetch_add(lp<float>(L_CS + (f & 3) * 4), cs, __ATOMIC_RELAXED, __HIP_MEMORY_SCOPE_WORKGROUP);
        *lp<u32x4>(L_CST + lane * 16) = (u32x4){wr[0] | wr[1] << 16, wr[2] | wr[3] << 16, wr[4] | wr[5] << 16, wr[6] | wr[7] << 16};
    }
    if (w < 2) {
        u32 c[4];
        if (w == 0) column4(P.w_down, lane, c);
        else column4(P.w_up, lane, c);
        if (lane < 4)
            *lp<u32x4>(L_W4 + w * 64 + lane * 16) =
                (u32x4){c[0] | c[1] << 16, c[2] | c[3] << 16, ((c[0] & 0x7fffu) ? 0x80u : 0u) | ((c[1] & 0x7fffu) ? 0x800000u : 0u),
                        ((c[2] & 0x7fffu) ? 0x80u : 0u) | ((c[3] & 0x7fffu) ? 0x800000u : 0u)};
    }
    u32x4 gs = {0u, 0u, 0u, 0u};
#pragma unroll 1
    for (int i = 1; i >= 0; --i) {
        const u32 e = 2 * tid + i;
        float rbf[8];
#pragma unroll
        for (int r = 0; r < 8; ++r) rbf[r] = radial(i ? hi2f(in.dd) : lo2f(in.dd), wf(P.centers, r));
        u32x4 pk;
#pragma unroll
        for (int j = 0; j < 4; ++j) pk[j] = pk_bf16(rbf[2 * j], rbf[2 * j + 1]);
        const f32x16 A = mfma32(wa, pk);
        u32 gm = 0;
#pragma unroll
        for (int d = 0; d < 4; ++d) gm = max(gm, __float_as_uint(fabsf(A[d]) + fabsf(A[4 + d]) + fabsf(A[8 + d])));
        const int ex = (int)(__float_as_uint(__uint_as_float(gm) * 1.01f) >> 23);
        ok &= ex < 170;
        const int ke = max(ex, 64) - 126;
        const float sc = __uint_as_float((u32)(127 - ke) << 23);
#pragma unroll
        for (int j = 0; j < 4; ++j) pk[j] = pk_bf16(rbf[2 * j] * sc, rbf[2 * j + 1] * sc);
        if (e < OSL) *lp<u32x4>(L_RBF + e * 16) = pk;
        const float up = __uint_as_float((u32)(127 + ke) << 23);
        ok &= fabsf(A[12]) + fabsf(A[13]) + fabsf(A[14]) + fabsf(A[15]) < 1e18f;
        gs = (u32x4){pk_bf16(A[12] * up, A[13] * up), pk_bf16(A[14] * up, A[15] * up), gs.x, gs.y};
    }
    if (!ok) *lp<u32>(L_FAIL) = 1;
    return gs;
}

constexpr u32 F_INIT = 0, F_TAB = 32, F_DONE = F_TAB + 16 * 32;
__device__ __forceinline__ void flag_set(const Params& P, u32 i, u32 v) {
    __hip_atomic_store(P.flags + i, v, __ATOMIC_RELAXED, __HIP_MEMORY_SCOPE_AGENT);
}
__device__ __forceinline__ u32 flag_get(const Params& P, u32 i) {
    return __hip_atomic_load(P.flags + i, __ATOMIC_RELAXED, __HIP_MEMORY_SCOPE_AGENT);
}
__device__ __forceinline__ void bump_init(const Params& P, u32 b, u32 lane) {
    if ((b & 15) == 0 && lane == 0) flag_set(P, F_INIT + (b >> 4), *lp<u32>(L_SNAP + (b >> 4) * 4) + 1 + lds_add(L_BUMP, 1u));
}

__device__ __forceinline__ void set_prio(int p) {
    if (p <= 0) __builtin_amdgcn_s_setprio(0);
    if (p == 1) __builtin_amdgcn_s_setprio(1);
    if (p == 2) __builtin_amdgcn_s_setprio(2);
    if (p >= 3) __builtin_amdgcn_s_setprio(3);
}

__device__ __forceinline__ u32x4 partition(const Params& P, u32 b, u32 tid, u32 w) {
    const u32 lane = tid & 63;
    const bool act = tid < ACT;
    auto r_i = raw_rsrc(P.idx_in, kT * 4);
    auto r_o = raw_rsrc(P.idx_out, kT * 4);
    auto r_a = raw_rsrc(P.angle, kT * 2);
    auto r_m = raw_rsrc(P.m + (size_t)b * OSL * 4, OSL * 8);
    auto r_s = raw_rsrc(P.rec, WS_FLAGS);
    const u32 o16 = act ? tid * 16 : OOB;
    const u32 o8 = act ? tid * 8 : OOB;

    u32x4 vo[QPT], vi[QPT];
    u32x2 va[QPT];
    u32 key[QPT * 4], pos[QPT * 4];
    u32 cbits = 0;

    auto loads = [&](int n0, int n1) {
#pragma unroll
        for (int n = n0; n < n1; ++n) {
            const int tl = n / (3 * QPT), x = n % (3 * QPT), j = x < 2 * QPT ? x / 2 : x - 2 * QPT;
            const u32 s = ((tl * QPT + j) * NWG + blockIdx.x) * ACT;
            if (x >= 2 * QPT) va[j] = __builtin_amdgcn_raw_buffer_load_b64(r_a, o8, s * 8, AUX_NT);
            else if (x & 1) vi[j] = __builtin_amdgcn_raw_buffer_load_b128(r_i, o16, s * 16, AUX_NT);
            else vo[j] = __builtin_amdgcn_raw_buffer_load_b128(r_o, o16, s * 16, AUX_NT);
        }
        __builtin_amdgcn_sched_barrier(0);
    };
    u32 one = 1, five = 5;
    asm volatile("" : "+v"(one), "+v"(five));
    auto hist_quad = [&](int j) {
#pragma unroll
        for (int r = 0; r < 4; ++r) {
            const u32 o = vo[j][r], i = vi[j][r];
            const u32 sl = mulhi24(o, MAGIC_O), q = mulhi24(i, MAGIC_I);
            u32 il;
            u64 wide;
            asm("v_mad_u64_u32 %0, vcc, %1, %2, 8" : "=v"(wide) : "v"(sl), "s"(0x80000000u - 8 * OSL) : "vcc");
            asm("v_mad_i32_i24 %0, %1, %2, %3" : "=v"(il) : "v"(q), "s"(-INR), "v"(i));
            pos[j * 4 + r] = (q << 10) + (sl << 2);
            key[j * 4 + r] = (il << 14) | ((u32)wide + (o << 3));
        }
        if (act) {
#pragma unroll
            for (int r = 0; r < 4; ++r) lds_add(L_HIST + pos[j * 4 + r], one);
        }
        asm volatile("" : "+v"(key[j * 4]), "+v"(key[j * 4 + 1]), "+v"(key[j * 4 + 2]), "+v"(key[j * 4 + 3]),
                          "+v"(pos[j * 4]), "+v"(pos[j * 4 + 1]), "+v"(pos[j * 4 + 2]), "+v"(pos[j * 4 + 3]));
        __builtin_amdgcn_sched_barrier(0);
    };
    u32x4 im[NST];
    auto image_read = [&]() {
#pragma unroll
        for (int j = 0; j < NST; ++j) im[j] = *lp<u32x4>(L_IMG + min(tid * 16 + j * 16384, (u32)(IMG_BYTES - 16)));
    };
    auto scan = [&](int tl) -> u32 {
        const u32 c = *lp<u32>(L_HIST + tid * 4);
        __builtin_amdgcn_sched_barrier(0);
        if (tl == 0) *lp<u32>(L_HIST + tid * 4) = 0;
        else image_read();
        __builtin_amdgcn_sched_barrier(0);
        const u32 cp = max(c, 1u);
        const u32 inc = wave_incl_scan(cp);
        if (lane == 63) *lp<u32>(L_WSUM + w * 4) = inc;
        __syncthreads();
        const u32 ws = *lp<u32>(L_WSUM + (tid & 15) * 4);
        const u32 wi = half_incl_scan(lane < 16 ? ws : 0u);
        const u32 total = __builtin_amdgcn_readlane(wi, 15);
        const u32 start = __builtin_amdgcn_readlane(wi, w) - __builtin_amdgcn_readlane(ws, w) + inc - cp;
        *lp<u32>(L_CUR + tid * 4) = L_IMG + start * 5 - 6;
        if (c == 0) *lp<u32>(L_IMG + ((start * 5 + 2) & ~3u)) = 0;
        if (tid == 0) *lp<u32>(L_IMG + ((total * 5 + 2) & ~3u)) = 0;
        const u32 ta = L_TAB + tl * TAB_BYTES + TAB_OFF + ((tid & 255) * KIN + ((tid >> 8) & 1) * 2 + (tid >> 9)) * 2;
        *lp<u16>(ta) = (u16)start;
        if ((tid & 255) == 255) *lp<u16>(ta + KIN * 2) = (u16)(start + cp);
        __syncthreads();
        return total;
    };
    auto place = [&](int j0, int j1) {
        if (act) {
#pragma unroll
            for (int r = 4 * j0; r < 4 * j1; ++r) pos[r] = lds_add(L_CUR + pos[r], five);
        }
        __builtin_amdgcn_sched_barrier(0);
    };
    u16x2 abias;
    auto encode = [&](int j) {
#pragma unroll
        for (int h = 0; h < 2; ++h) {
            const u32 raw = va[j][h] | 0x80008000u;
            const u16x2 cw = __builtin_elementwise_sub_sat(__builtin_bit_cast(u16x2, raw), abias);
            va[j][h] = __builtin_bit_cast(u32, cw);
        }
        cbits |= va[j].x | va[j].y;
        asm volatile("" : "+v"(va[j]), "+v"(cbits));
    };
    auto put_quad = [&](int j) {
        if (act) {
#pragma unroll
            for (int r = 0; r < 4; ++r) {
                const u32 p = pos[j * 4 + r], code = va[j][r >> 1] >> (16 * (r & 1));
                *lp<u32>((p & ~3u) + 8) = key[j * 4 + r] + ((code >> 8) & 0xffu);
                *lp<u8>(p + __builtin_amdgcn_ubfe(0x0a0e0107u, p << 3, 8u)) = (u8)code;
            }
        }
        __builtin_amdgcn_sched_barrier(0);
    };
    auto image_store = [&](int tl, u32 total, int j0, int j1) {
        auto r_w = raw_rsrc(P.rec + (size_t)(b * TPW + tl) * TILE_BYTES, (total * 5 + 16 + 127) & ~127u);
#pragma unroll
        for (int j = j0; j < j1; ++j) __builtin_amdgcn_raw_buffer_store_b128(im[j], r_w, tid * 16 + j * 16384, 0, AUX_SC1);
    };

    constexpr int kLd[20] = {2, 5, 7, 9, 10, 13, 15, 16, 17, 18, 19, 19, 20, 21, 22, 24, 25, 25, 30, 30};
    static_assert(QPT == 5 && kLd[2] >= 4 && kLd[4] >= 10 && kLd[6] == 15 && kLd[14] <= 25 && kLd[15] >= 17 &&
                  kLd[16] >= 25 && kLd[19] == 30, "every load precedes its use and follows the last use of its register");
    set_prio(3);
    if (w == 15) {
        if (lane == 0 || (lane == 1 && (b & 15) == 0)) flag_set(P, lane ? F_TAB + (b >> 4) * 32 : F_DONE + b * 32, 0u);
        const u32 snap = flag_get(P, F_INIT + (lane & 15));
        u64 pc;
        asm volatile("s_getpc_b64 %0" : "=s"(pc));
        const u32 c = blockIdx.x >> 3;
        const u32 code = __builtin_amdgcn_raw_buffer_load_b32(raw_rsrc((const void*)(pc & ~63ull), CODE_BYTES),
                                                              ((c & 15) * 64 + lane) * 64, 0, 0);
        *lp<u32>(L_HIST + tid * 4) = 0;
        __syncthreads();
        u32 sink;
        asm volatile("s_load_dword %0, %1, 0x0\n\ts_load_dword %0, %2, 0x0\n\ts_load_dword %0, %3, 0x0\n\t"
                     "s_load_dword %0, %4, 0x0\n\ts_load_dword %0, %5, 0x0\n\ts_load_dword %0, %6, 0x0\n\t"
                     "s_load_dword %0, %7, 0x0\n\ts_load_dword %0, %8, 0x0\n\ts_load_dword %0, %9, 0x0\n\t"
                     "s_load_dword %0, %10, 0x0\n\ts_load_dword %0, %11, 0x0\n\ts_load_dword %0, %12, 0x0\n\t"
                     "s_load_dword %0, %13, 0x0\n\ts_load_dword %0, %14, 0x0\n\ts_load_dword %0, %15, 0x0\n\t"
                     "s_waitcnt vmcnt(0) lgkmcnt(0)"
                     : "=&s"(sink)
                     : "s"(P.idx_out + c * (kT / 32)), "s"(P.idx_in + c * (kT / 32)), "s"(P.angle + c * (kT / 32)),
                       "s"(P.m + b * OSL * 4), "s"(P.dist + b * OSL), "s"(P.w_sbf), "s"(P.w_rbf), "s"(P.w_up), "s"(P.w_down),
                       "s"(P.centers), "s"(P.freq), "s"(P.m + c * (kE / 32) * 4), "s"(P.rec + (b * TPW + 1) * TILE_BYTES - 128),
                       "s"(P.rec + (c * 16 + 1) * TILE_BYTES - 128), "s"(P.rec + (c * 16 + 9) * TILE_BYTES - 128)
                     : "memory");
        asm volatile("" :: "v"(code));
        *lp<u32>(L_SNAP + (lane & 15) * 4) = snap;
        loads(0, kLd[0]);
    } else {
        loads(0, kLd[0]);
        if (tid <= NBIN) *lp<u32>(L_CNT + tid * 4) = L_BINS + tid * BSTR;
        if (tid < 20) *lp<u32>(L_MISC + tid * 4) = 0;
        if (tid < 64) *lp<u32>(L_GO + tid * 4) = 0;
        *lp<u32>(L_HIST + tid * 4) = 0;
        __syncthreads();
    }
    loads(kLd[0], kLd[1]);
    const u32x4 mrow = __builtin_amdgcn_raw_buffer_load_b128(r_m, tid * 16, 0, 0);
    const EdgeIn ein = edge_tables_load(P, b * OSL, tid);
    __builtin_amdgcn_sched_barrier(0);
#pragma unroll
    for (int j = 0; j < QPT; ++j) {
        set_prio(QPT - 2 - j);
        loads(kLd[1 + j], kLd[2 + j]);
        hist_quad(j);
        if (j == 1 && w == 15) bump_init(P, b, lane);
    }
    loads(kLd[6], kLd[7]);
    const int abase = angle_base(wf(P.freq, 0), wf(P.freq, 1), wf(P.freq, 2));
    abias = (u16)(0x8000 + max(abase, 0) * 128);
    if (w < EW) {
        u32 c[4];
        column4(P.w_down, lane, c);
        const u32x2 wd = {c[0] | c[1] << 16, c[2] | c[3] << 16};
        const f32x4 x = mfma4(wd, (u32x2){mrow.x, mrow.y}), y = mfma4(wd, (u32x2){mrow.z, mrow.w});
#pragma unroll
        for (int d = 0; d < 4; ++d) {
            const u32 mx = wave_max_u32(max(__float_as_uint(fabsf(x[d])), __float_as_uint(fabsf(y[d]))));
            const float sm = wave_sum_f32(fabsf(x[d]) + fabsf(y[d]));
            if (lane == 0) {
                *lp<u32>(L_WSTAT + (2 * d * 16 + w) * 4) = mx;
                *lp<float>(L_WSTAT + ((2 * d + 1) * 16 + w) * 4) = sm;
            }
        }
    }
    __syncthreads();
    const u32 total0 = scan(0);
    set_prio(3);
    loads(kLd[7], kLd[8]);
    place(0, 2);
    loads(kLd[8], kLd[9]);
    place(2, QPT);
#pragma unroll
    for (int j = 0; j < QPT; ++j) {
        set_prio(j < 3 ? 2 : 1);
        loads(kLd[9 + j], kLd[10 + j]);
        encode(j);
        put_quad(j);
    }
    if (w == 1 && lane < 8) {
        u32 mx = 0;
        float sm = 0.f;
#pragma unroll
        for (int i = 0; i < EW; ++i) {
            const u32 v = *lp<u32>(L_WSTAT + (lane * 16 + i) * 4);
            mx = max(mx, v);
            sm += __uint_as_float(v);
        }
        *lp<u32>(L_STAT + lane * 4) = (lane & 1) ? __float_as_uint(sm) : mx;
    }
    u32 seen = __builtin_amdgcn_raw_buffer_load_b32(raw_rsrc(P.flags + F_INIT, w == 15 ? 64 : 0), (lane & 15) * 4, 0, AUX_SC1);
#pragma unroll
    for (int j = 0; j < QPT; ++j) {
        set_prio(j < 2 ? 1 : 0);
        loads(kLd[14 + j], kLd[15 + j]);
        if (j == 1 && tid == 960 && (b & 7) == 0) flag_set(P, F_DONE + b * 32, 1u);
        hist_quad(j);
    }
    bool inited = __builtin_amdgcn_ballot_w64(seen == *lp<u32>(L_SNAP + (lane & 15) * 4)) == 0;
    __syncthreads();

    const u32 to0 = tid * 16 < TAB_BYTES ? b * (TPW * TILE_BYTES) + IMG_BYTES + tid * 16 : OOB;
    const u32 to1 = tid * 16 - TAB_BYTES < 32 ? WS_STATS + b * 32 + (tid * 16 - TAB_BYTES) : to0 + TILE_BYTES;
    u32 sent = 0;
    auto refresh = [&]() {
        if (w == 0) {
            u32 keep;
            asm volatile("s_mov_b32 %0, m0\n\ts_mov_b32 m0, %2\n\ts_nop 2\n\tglobal_load_lds_dword %1, %3 sc1\n\ts_mov_b32 m0, %0"
                         : "=&s"(keep) : "v"((F_DONE + (lane >> 1) * 8 * 32) * 4), "s"(L_GO), "s"(P.flags) : "memory");
        }
    };
    auto send = [&](bool force) {
        if (sent == 0) {
            if (!force && __builtin_amdgcn_ballot_w64(*(volatile LDS u32*)lp<u32>(L_GO + lane * 4) == 0) != 0) {
                refresh();
                return;
            }
            if (w < 3) {
                __builtin_amdgcn_raw_buffer_store_b128(*lp<u32x4>(L_TAB + tid * 16), r_s, to0, 0, AUX_SC1);
                __builtin_amdgcn_raw_buffer_store_b128(*lp<u32x4>(L_TAB + TAB_BYTES + tid * 16), r_s, to1, 0, AUX_SC1);
            }
            image_store(0, total0, 0, 1);
        } else if (sent == 1) {
            image_store(0, total0, 1, 2);
        } else if (sent == 2) {
            image_store(0, total0, 2, 4);
        } else if (sent == 3) {
            image_store(0, total0, 4, NST);
        }
        ++sent;
    };
    refresh();
    const u32 total1 = scan(1);
    set_prio(3);
    place(0, 2);
#pragma unroll
    for (int j = 0; j < QPT; ++j) encode(j);
    if (abase < 0 || (cbits & 0xf800f800u) != 0) *lp<u32>(L_BAD) = 1;
    __builtin_amdgcn_sched_barrier(0);
#pragma unroll
    for (int j = 0; j < QPT; ++j) {
        set_prio(QPT - 1 - j);
        send(j >= 3);
        __builtin_amdgcn_sched_barrier(0);
        if (j + 2 < QPT) place(j + 2, j + 3);
        put_quad(j);
    }
    send(true);
    send(true);
    if (w < 3) asm volatile("s_waitcnt vmcnt(%0)" :: "n"(NST) : "memory");
    __syncthreads();
    if (w == 15) {
#pragma unroll 1
        for (u32 spins = 0; !inited && spins < SPIN_MAX; ++spins) {
            bump_init(P, b, lane);
            __builtin_amdgcn_s_sleep(16);
            inited = __builtin_amdgcn_ballot_w64(flag_get(P, F_INIT + (lane & 15)) == *lp<u32>(L_SNAP + (lane & 15) * 4)) == 0;
        }
        if (!inited) *lp<u32>(L_LOST) = 1;
        set_prio(3);
        if (lane == 0) __hip_atomic_fetch_add(P.flags + F_TAB + (b >> 4) * 32, 1u, __ATOMIC_RELAXED, __HIP_MEMORY_SCOPE_AGENT);
        set_prio(0);
    }
    image_read();
    __syncthreads();
    image_store(1, total1, 0, NST);
    __builtin_amdgcn_sched_barrier(0);
    u32x4 gs = {0u, 0u, 0u, 0u};
    if (w < EW) gs = edge_tables(P, ein, tid, w);
    *lp<u32x4>(L_ACC0 + tid * 16) = (u32x4){0u, 0u, 0u, 0u};
    if (tid < OSL - 1024) *lp<u32x4>(L_ACC0 + (tid + 1024) * 16) = (u32x4){0u, 0u, 0u, 0u};
    asm volatile("s_waitcnt vmcnt(0)" ::: "memory");
    __syncthreads();
    if (tid == 0) flag_set(P, F_DONE + b * 32, 2u + *lp<u32>(L_BAD));
    return gs;
}

__device__ __forceinline__ void accumulate_generic(const CONST Params& P, u32 b, u32 tid) {
    const u32 lane = tid & 63, w = __builtin_amdgcn_readfirstlane(tid >> 6), e0 = b * OSL;
    constexpr u32 G_ACC = OSL * 48;
    for (u32 e = tid; e < OSL; e += 1024) {
        const float dist = bf2f(P.dist[e0 + e]);
        f32x4 a[3] = {};
#pragma unroll 1
        for (int r = 0; r < 8; ++r) {
            const float x = radial(dist, wf(P.centers, r));
#pragma unroll
            for (int j = 0; j < 12; ++j) a[j >> 2][j & 3] += x * wf(P.w_sbf + r * 12, j);
        }
#pragma unroll
        for (int l = 0; l < 3; ++l) *lp<f32x4>((l * OSL + e) * 16) = a[l];
#pragma unroll
        for (int d = 0; d < 4; ++d) *lp<double>(G_ACC + (d * OSL + e) * 8) = 0.0;
    }
    const float fr0 = bf2f(P.freq[0]), fr1 = bf2f(P.freq[1]), fr2 = bf2f(P.freq[2]);
    __syncthreads();
    auto r_m = raw_rsrc(P.m, kE * 8);
    const u32 queue = OSL * 80 + w * 1280;
    u32 n = 0;
#pragma unroll 1
    for (u32 q = tid; q < kT / 4 + 1024; q += 1024) {
        const bool more = q < kT / 4;
        if (more) {
            const u32x4 o = ((const u32x4*)P.idx_out)[q];
#pragma unroll
            for (int r = 0; r < 4; ++r) {
                const bool hit = o[r] - e0 < OSL;
                const u64 mask = __builtin_amdgcn_ballot_w64(hit);
                if (hit) *lp<u32>(queue + (n + __builtin_amdgcn_mbcnt_hi(mask >> 32, __builtin_amdgcn_mbcnt_lo(mask, 0u))) * 4) = q * 4 + r;
                n += __builtin_popcountll(mask);
            }
        }
#pragma unroll 1
        while (n >= (more ? 64u : 1u)) {
            const u32 take = min(n, 64u);
            n -= take;
            if (lane < take) {
                const u32 t = *lp<u32>(queue + (n + lane) * 4);
                const u32 a8 = (P.idx_out[t] - e0) * 8;
                const float a = bf2f(P.angle[t]);
                const u32x2 g = __builtin_amdgcn_raw_buffer_load_b64(r_m, P.idx_in[t] * 8, 0, 0);
                f32x4 gate = {0.f, 0.f, 0.f, 0.f};
#pragma unroll 1
                for (int l = 0; l < 3; ++l)
                    gate += *lp<f32x4>(l * OSL * 16 + 2 * a8) * cosf(rbf16((l == 0 ? fr0 : (l == 1 ? fr1 : fr2)) * a));
#pragma unroll
                for (int d = 0; d < 4; ++d) {
                    const float md = lo2f(g.x) * wf(P.w_down, d) + hi2f(g.x) * wf(P.w_down, 4 + d) +
                                     lo2f(g.y) * wf(P.w_down, 8 + d) + hi2f(g.y) * wf(P.w_down, 12 + d);
                    __hip_atomic_fetch_add(lp<double>(G_ACC + d * OSL * 8 + a8), (double)(md * gate[d]), __ATOMIC_RELAXED,
                                           __HIP_MEMORY_SCOPE_WORKGROUP);
                }
            }
        }
    }
    __syncthreads();
    for (u32 e = tid; e < OSL; e += 1024) {
        const float dist = bf2f(P.dist[e0 + e]);
        float ag[4], o[4], g[4] = {0.f, 0.f, 0.f, 0.f};
#pragma unroll 1
        for (int r = 0; r < 8; ++r) {
            const float x = radial(dist, wf(P.centers, r));
#pragma unroll
            for (int d = 0; d < 4; ++d) g[d] += x * wf(P.w_rbf + r * 4, d);
        }
        const u32x2 mr = ((const u32x2*)P.m)[e0 + e];
        const float mv[4] = {lo2f(mr.x), hi2f(mr.x), lo2f(mr.y), hi2f(mr.y)};
#pragma unroll
        for (int d = 0; d < 4; ++d) ag[d] = rbf16((float)*lp<double>(G_ACC + (d * OSL + e) * 8));
#pragma unroll
        for (int d = 0; d < 4; ++d) {
            float u = 0.f;
#pragma unroll
            for (int k = 0; k < 4; ++k) u += ag[k] * wf(P.w_up, k * 4 + d);
            o[d] = mv[d] + rbf16(rbf16(u) * rbf16(g[d]));
        }
        ((u32x2*)P.out)[e0 + e] = (u32x2){pk_bf16(o[0], o[1]), pk_bf16(o[2], o[3])};
    }
}

__device__ __forceinline__ u32 flatten_setup(u32 st, u32 en, u32 tbase, u32 scr, u32 lane) {
    const u32 first = st >> 1, len = ((en + 1) >> 1) - first;
    const u32 inc = half_incl_scan(len);
    const u32 l5 = lane & 31;
    const u32 sb = scr + (lane >> 5) * SCR_B;
    *lp<u32>(sb + (l5 < 16 ? l5 * 4 : 64 + 128)) = l5 < 16 ? 0u : OOB;
    *lp<u32>(sb + 64 + l5 * 4) = tbase + (first - (inc - len)) * 10;
    const u32 last = inc - 1;
    __hip_atomic_fetch_or(lp<u32>(sb + min(last >> 5, 15u) * 4), 1u << (last & 31), __ATOMIC_RELAXED, __HIP_MEMORY_SCOPE_WAVEFRONT);
    return inc;
}

__device__ __forceinline__ void first_addresses(u32 (&dl)[GSL], u32 sb, u32 lane) {
    const u32 mk = *lp<u32>(sb + (lane & 15) * 4);
    u32 tab = sb + 64;
#pragma unroll
    for (int j = 0; j < GSL; ++j) {
        const u32 lo = __builtin_amdgcn_readlane(mk, 2 * j), hi = __builtin_amdgcn_readlane(mk, 2 * j + 1);
        const u32 sg = __builtin_amdgcn_mbcnt_hi(hi, __builtin_amdgcn_mbcnt_lo(lo, 0u));
        dl[j] = *lp<u32>(tab + sg * 4) + lane * 10;
        tab += (__builtin_popcount(lo) + __builtin_popcount(hi)) * 4;
        asm volatile("" : "+s"(tab));
    }
}

__device__ __forceinline__ void accumulate(const Params& P, u32 b, u32 tid, u32 w, u32x4 gs) {
    u32 lane = tid & 63;
    const u32 e0 = b * OSL;
    bool ok;
    u32 nsum = 0;
    u32x4 mrow;
    set_prio(3);
    {
        const bool trust = __builtin_amdgcn_readfirstlane(*lp<u32>(L_LOST)) == 0;
        bool have = false;
#pragma unroll 1
        for (u32 spins = 0; trust && !have && spins < SPIN_MAX; ++spins) {
            if (spins) __builtin_amdgcn_s_sleep(16);
            if ((spins & 3) == 3) bump_init(P, b, lane);
            have = __builtin_amdgcn_readfirstlane(flag_get(P, F_TAB + w * 32)) == 16;
        }
        __builtin_amdgcn_fence(__ATOMIC_ACQUIRE, "workgroup");
        const u32 tile = w * 32 + (lane & 31);
        const u32x3 seg = __builtin_amdgcn_raw_buffer_load_b96(
            raw_rsrc(P.rec, WS_STATS), tile * TILE_BYTES + IMG_BYTES + TAB_OFF + (b * 2 + (lane >> 5)) * 4, 0, AUX_SC1);
        const u32x2 sv = __builtin_amdgcn_raw_buffer_load_b64(raw_rsrc(P.rec + WS_STATS, NWG * 32), tid * 8, 0, AUX_SC1);
        const u32 fd = F_DONE + (w * 16 + (lane & 15)) * 32;
        u32 done = flag_get(P, fd);
        const u32 scr = L_SCR + w * SCR_W;
        const u32 ia = flatten_setup(seg.x & 0xffffu, seg.z & 0xffffu, tile * TILE_BYTES, scr, lane);
        u32 dl[GSL];
        first_addresses(dl, scr, lane);
        asm volatile("" : "+v"(dl[0]), "+v"(dl[1]), "+v"(dl[2]), "+v"(dl[3]), "+v"(dl[4]), "+v"(dl[5]));
#pragma unroll 1
        for (u32 spins = 0; have && __builtin_amdgcn_ballot_w64(done < 2) != 0; ++spins) {
            if (spins) __builtin_amdgcn_s_sleep(8);
            if (done < 2) done = flag_get(P, fd);
            have = spins < SPIN_MAX;
        }
        __builtin_amdgcn_fence(__ATOMIC_ACQUIRE, "workgroup");
        const u64 pr = (u64)P.rec, pm = (u64)P.m;
        auto r_rec = raw_rsrc(P.rec, have ? WS_STATS : 0u);
        u32x4 raw[GSL];
#pragma unroll
        for (int j = 0; j < GSL; ++j) {
            const u32x3 v = __builtin_amdgcn_raw_buffer_load_b96(r_rec, (dl[j] & ~3u) + j * 640, 0, AUX_SC1);
            raw[j] = (u32x4){v.x, v.y, v.z, dl[j] & 2u};
        }
        __builtin_amdgcn_sched_barrier(0);
        set_prio(0);
        const u32 ib = flatten_setup(seg.x >> 16, seg.z >> 16, tile * TILE_BYTES, scr + 2 * SCR_B, lane);
        ok = have & (max(ia, ib) <= (GSL + 1) * 64) & (done == 2);
        const u32 nrec = __builtin_amdgcn_ballot_w64(!ok) == 0 ? WS_STATS : 0u;
        const u64 ba = __builtin_amdgcn_ballot_w64(ia > GSL * 64), bb = __builtin_amdgcn_ballot_w64(ib > GSL * 64);
        const u32 big = ((u32)ba >> 31) | (u32)(ba >> 63) << 1 | ((u32)bb >> 31) << 2 | (u32)(bb >> 63) << 3;
        const u32 cls = (b & 1) ? 0x018u : 0x180u;
        {
            u32 mx = max(sv.x, dpp0<0x114, 0xf>(sv.x));
            float sm = __uint_as_float(sv.y) + dpp0f<0x114, 0xf>(__uint_as_float(sv.y));
            mx = max(mx, dpp0<0x118, 0xf>(mx));
            sm += dpp0f<0x118, 0xf>(sm);
            if ((lane & 12) == 12) {
                __hip_atomic_fetch_max(lp<u32>(L_MX + (lane & 3) * 4), __float_as_uint(__uint_as_float(mx) * 1.01f), __ATOMIC_RELAXED,
                                       __HIP_MEMORY_SCOPE_WORKGROUP);
                __hip_atomic_fetch_add(lp<float>(L_SM + (lane & 3) * 4), sm, __ATOMIC_RELAXED, __HIP_MEMORY_SCOPE_WORKGROUP);
            }
        }

        const u32 sl = w * S + min(lane, (u32)(S - 1));
        const u32 lane10 = lane * 10;
        const u32 rbase = L_BINS + w * (S * BSTR) + lane * 8;
        const u32 cntr = L_CNT + min(sl, (u32)NBIN) * 4;
        const u32 cnti = L_BINS + min(sl, (u32)NBIN) * BSTR;
        const u32 csub = cnti + (max(sl, (u32)NBIN) - NBIN) * (63 * 8);
        const u32 ovd = w == 15 ? BSTR - 63 * 8 : 0u;
        const u32 mka = scr + (lane & 15) * 4;
        constexpr float kInv2Pi = 0.15915494309189535f;
        float f0 = wf(P.freq, 0), f1 = wf(P.freq, 1), f2 = wf(P.freq, 2);
        const int abase = angle_base(f0, f1, f2);
        f0 = ldexpf(f0, abase) * kInv2Pi, f1 = ldexpf(f1, abase) * kInv2Pi, f2 = ldexpf(f2, abase) * kInv2Pi;
        const u64 po = pm + (u64)e0 * 8;
        int chk = 0;
    asm volatile(
        ".p2align 6\n"
        "s_waitcnt lgkmcnt(0)\n"
        "s_nop 4\n"
        "s_mov_b64 s[36:37], %[pr]\n"
        "s_and_b32 s37, s37, 0xffff\n"
        "s_mov_b32 s38, %[nrec]\n"
        "s_mov_b32 s39, 0x20000\n"
        "s_mov_b64 s[40:41], %[pm]\n"
        "s_and_b32 s41, s41, 0xffff\n"
        "s_or_b32 s41, s41, 0x80000\n"
        "s_mov_b32 s42, 320000\n"
        "s_mov_b32 s43, 0x20000\n"
        "s_mov_b32 s53, %[scr]\n"
        "v_readfirstlane_b32 s44, v72\n"
        "v_readfirstlane_b32 s45, v73\n"
        "v_readfirstlane_b32 s46, v74\n"
        "s_mov_b32 s47, 2464\n"
        "s_mov_b32 s56, 0x3f000000\n"
        "s_mov_b32 s48, 0x2400000\n"
        "s_mov_b32 s49, 0x20400000\n"
        "s_mov_b32 s50, 0x20400000\n"
        "s_mov_b32 s51, 0x20400000\n"
        "s_mov_b32 s92, 0x4010c0c\n"
        "s_mov_b32 s93, 0x4000c0c\n"
        "v_mov_b32 v114, 0x1000008\n"
        "v_mov_b32 v88, 0\n"
        "v_mov_b32 v89, 0\n"
        "v_mov_b32 v90, 0\n"
        "v_mov_b32 v91, 0\n"
        "v_mov_b32 v115, 20768\n"
        "ds_read_b32 v107, v11 offset:208\n"
        "v_cmp_ne_u32_e32 vcc, 0, v17\n"
        "v_and_b32 v110, 0x7ffc0000, v15\n"
        "v_cmp_class_f32_e64 s[60:61], v15, %[cls]\n"
        "v_cndmask_b32_e32 v17, v14, v16, vcc\n"
        "v_mul_hi_u32 v110, v110, s47\n"
        "v_cndmask_b32_sdwa v16, v16, v14, vcc dst_sel:DWORD dst_unused:UNUSED_PAD src0_sel:WORD_0 src1_sel:WORD_1\n"
        "v_and_b32 v111, 0x7ffc0000, v17\n"
        "v_cmp_class_f32_e64 s[62:63], v17, %[cls]\n"
        "v_mul_hi_u32 v111, v111, s47\n"
        "v_and_b32 v110, -4, v110\n"
        "v_and_b32 v111, -4, v111\n"
        "s_mov_b64 exec, s[60:61]\n"
        "ds_add_rtn_u32 v38, v110, v114 offset:20016\n"
        "s_mov_b64 exec, s[62:63]\n"
        "ds_add_rtn_u32 v39, v111, v114 offset:20016\n"
        "s_mov_b64 exec, -1\n"
        "v_perm_b32 v14, v15, v16, s92\n"
        "v_perm_b32 v16, v17, v16, s93\n"
        "v_and_b32 v14, 0x7ff0000, v14\n"
        "v_and_b32 v16, 0x7ff0000, v16\n"
        "v_cmp_ne_u32_e32 vcc, 0, v21\n"
        "v_and_b32 v110, 0x7ffc0000, v19\n"
        "v_cmp_class_f32_e64 s[64:65], v19, %[cls]\n"
        "v_cndmask_b32_e32 v21, v18, v20, vcc\n"
        "v_mul_hi_u32 v110, v110, s47\n"
        "v_cndmask_b32_sdwa v20, v20, v18, vcc dst_sel:DWORD dst_unused:UNUSED_PAD src0_sel:WORD_0 src1_sel:WORD_1\n"
        "v_and_b32 v111, 0x7ffc0000, v21\n"
        "v_cmp_class_f32_e64 s[66:67], v21, %[cls]\n"
        "v_mul_hi_u32 v111, v111, s47\n"
        "v_and_b32 v110, -4, v110\n"
        "v_and_b32 v111, -4, v111\n"
        "s_mov_b64 exec, s[64:65]\n"
        "ds_add_rtn_u32 v40, v110, v114 offset:20016\n"
        "s_mov_b64 exec, s[66:67]\n"
        "ds_add_rtn_u32 v41, v111, v114 offset:20016\n"
        "s_mov_b64 exec, -1\n"
        "v_perm_b32 v18, v19, v20, s92\n"
        "v_perm_b32 v20, v21, v20, s93\n"
        "v_and_b32 v18, 0x7ff0000, v18\n"
        "v_and_b32 v20, 0x7ff0000, v20\n"
        "v_cmp_ne_u32_e32 vcc, 0, v25\n"
        "v_and_b32 v110, 0x7ffc0000, v23\n"
        "v_cmp_class_f32_e64 s[68:69], v23, %[cls]\n"
        "v_cndmask_b32_e32 v25, v22, v24, vcc\n"
        "v_mul_hi_u32 v110, v110, s47\n"
        "v_cndmask_b32_sdwa v24, v24, v22, vcc dst_sel:DWORD dst_unused:UNUSED_PAD src0_sel:WORD_0 src1_sel:WORD_1\n"
        "v_and_b32 v111, 0x7ffc0000, v25\n"
        "v_cmp_class_f32_e64 s[70:71], v25, %[cls]\n"
        "v_mul_hi_u32 v111, v111, s47\n"
        "v_and_b32 v110, -4, v110\n"
        "v_and_b32 v111, -4, v111\n"
        "s_mov_b64 exec, s[68:69]\n"
        "ds_add_rtn_u32 v42, v110, v114 offset:20016\n"
        "s_mov_b64 exec, s[70:71]\n"
        "ds_add_rtn_u32 v43, v111, v114 offset:20016\n"
        "s_mov_b64 exec, -1\n"
        "v_perm_b32 v22, v23, v24, s92\n"
        "v_perm_b32 v24, v25, v24, s93\n"
        "v_and_b32 v22, 0x7ff0000, v22\n"
        "v_and_b32 v24, 0x7ff0000, v24\n"
        "v_cmp_ne_u32_e32 vcc, 0, v29\n"
        "v_and_b32 v110, 0x7ffc0000, v27\n"
        "v_cmp_class_f32_e64 s[72:73], v27, %[cls]\n"
        "v_cndmask_b32_e32 v29, v26, v28, vcc\n"
        "v_mul_hi_u32 v110, v110, s47\n"
        "v_cndmask_b32_sdwa v28, v28, v26, vcc dst_sel:DWORD dst_unused:UNUSED_PAD src0_sel:WORD_0 src1_sel:WORD_1\n"
        "v_and_b32 v111, 0x7ffc0000, v29\n"
        "v_cmp_class_f32_e64 s[74:75], v29, %[cls]\n"
        "v_mul_hi_u32 v111, v111, s47\n"
        "v_and_b32 v110, -4, v110\n"
        "v_and_b32 v111, -4, v111\n"
        "s_mov_b64 exec, s[72:73]\n"
        "ds_add_rtn_u32 v44, v110, v114 offset:20016\n"
        "s_mov_b64 exec, s[74:75]\n"
        "ds_add_rtn_u32 v45, v111, v114 offset:20016\n"
        "s_mov_b64 exec, -1\n"
        "v_perm_b32 v26, v27, v28, s92\n"
        "v_perm_b32 v28, v29, v28, s93\n"
        "v_and_b32 v26, 0x7ff0000, v26\n"
        "v_and_b32 v28, 0x7ff0000, v28\n"
        "v_cmp_ne_u32_e32 vcc, 0, v33\n"
        "v_and_b32 v110, 0x7ffc0000, v31\n"
        "v_cmp_class_f32_e64 s[76:77], v31, %[cls]\n"
        "v_cndmask_b32_e32 v33, v30, v32, vcc\n"
        "v_mul_hi_u32 v110, v110, s47\n"
        "v_cndmask_b32_sdwa v32, v32, v30, vcc dst_sel:DWORD dst_unused:UNUSED_PAD src0_sel:WORD_0 src1_sel:WORD_1\n"
        "v_and_b32 v111, 0x7ffc0000, v33\n"
        "v_cmp_class_f32_e64 s[78:79], v33, %[cls]\n"
        "v_mul_hi_u32 v111, v111, s47\n"
        "v_and_b32 v110, -4, v110\n"
        "v_and_b32 v111, -4, v111\n"
        "s_mov_b64 exec, s[76:77]\n"
        "ds_add_rtn_u32 v46, v110, v114 offset:20016\n"
        "s_mov_b64 exec, s[78:79]\n"
        "ds_add_rtn_u32 v47, v111, v114 offset:20016\n"
        "s_mov_b64 exec, -1\n"
        "v_perm_b32 v30, v31, v32, s92\n"
        "v_perm_b32 v32, v33, v32, s93\n"
        "v_and_b32 v30, 0x7ff0000, v30\n"
        "v_and_b32 v32, 0x7ff0000, v32\n"
        "v_cmp_ne_u32_e32 vcc, 0, v37\n"
        "v_and_b32 v110, 0x7ffc0000, v35\n"
        "v_cmp_class_f32_e64 s[80:81], v35, %[cls]\n"
        "v_cndmask_b32_e32 v37, v34, v36, vcc\n"
        "v_mul_hi_u32 v110, v110, s47\n"
        "v_cndmask_b32_sdwa v36, v36, v34, vcc dst_sel:DWORD dst_unused:UNUSED_PAD src0_sel:WORD_0 src1_sel:WORD_1\n"
        "v_and_b32 v111, 0x7ffc0000, v37\n"
        "v_cmp_class_f32_e64 s[82:83], v37, %[cls]\n"
        "v_mul_hi_u32 v111, v111, s47\n"
        "v_and_b32 v110, -4, v110\n"
        "v_and_b32 v111, -4, v111\n"
        "s_mov_b64 exec, s[80:81]\n"
        "ds_add_rtn_u32 v48, v110, v114 offset:20016\n"
        "s_mov_b64 exec, s[82:83]\n"
        "ds_add_rtn_u32 v49, v111, v114 offset:20016\n"
        "s_mov_b64 exec, -1\n"
        "v_perm_b32 v34, v35, v36, s92\n"
        "v_perm_b32 v36, v37, v36, s93\n"
        "v_and_b32 v34, 0x7ff0000, v34\n"
        "v_and_b32 v36, 0x7ff0000, v36\n"
        "s_mov_b64 exec, s[60:61]\n"
        "s_waitcnt lgkmcnt(11)\n"
        "v_cmpx_le_u32_e32 vcc, s56, v38\n"
        "ds_add_rtn_u32 v38, v115, v114\n"
        "s_mov_b64 exec, s[62:63]\n"
        "s_waitcnt lgkmcnt(11)\n"
        "v_cmpx_le_u32_e32 vcc, s56, v39\n"
        "ds_add_rtn_u32 v39, v115, v114\n"
        "s_mov_b64 exec, -1\n"
        "s_nop 1\n"
        "s_mov_b64 exec, s[64:65]\n"
        "s_waitcnt lgkmcnt(11)\n"
        "v_cmpx_le_u32_e32 vcc, s56, v40\n"
        "ds_add_rtn_u32 v40, v115, v114\n"
        "s_mov_b64 exec, s[66:67]\n"
        "s_waitcnt lgkmcnt(11)\n"
        "v_cmpx_le_u32_e32 vcc, s56, v41\n"
        "ds_add_rtn_u32 v41, v115, v114\n"
        "s_mov_b64 exec, -1\n"
        "s_nop 1\n"
        "s_mov_b64 exec, s[68:69]\n"
        "s_waitcnt lgkmcnt(11)\n"
        "v_cmpx_le_u32_e32 vcc, s56, v42\n"
        "ds_add_rtn_u32 v42, v115, v114\n"
        "s_mov_b64 exec, s[70:71]\n"
        "s_waitcnt lgkmcnt(11)\n"
        "v_cmpx_le_u32_e32 vcc, s56, v43\n"
        "ds_add_rtn_u32 v43, v115, v114\n"
        "s_mov_b64 exec, -1\n"
        "s_nop 1\n"
        "s_mov_b64 exec, s[72:73]\n"
        "s_waitcnt lgkmcnt(11)\n"
        "v_cmpx_le_u32_e32 vcc, s56, v44\n"
        "ds_add_rtn_u32 v44, v115, v114\n"
        "s_mov_b64 exec, s[74:75]\n"
        "s_waitcnt lgkmcnt(11)\n"
        "v_cmpx_le_u32_e32 vcc, s56, v45\n"
        "ds_add_rtn_u32 v45, v115, v114\n"
        "s_mov_b64 exec, -1\n"
        "s_nop 1\n"
        "s_mov_b64 exec, s[76:77]\n"
        "s_waitcnt lgkmcnt(11)\n"
        "v_cmpx_le_u32_e32 vcc, s56, v46\n"
        "ds_add_rtn_u32 v46, v115, v114\n"
        "s_mov_b64 exec, s[78:79]\n"
        "s_waitcnt lgkmcnt(11)\n"
        "v_cmpx_le_u32_e32 vcc, s56, v47\n"
        "ds_add_rtn_u32 v47, v115, v114\n"
        "s_mov_b64 exec, -1\n"
        "s_nop 1\n"
        "s_mov_b64 exec, s[80:81]\n"
        "s_waitcnt lgkmcnt(11)\n"
        "v_cmpx_le_u32_e32 vcc, s56, v48\n"
        "ds_add_rtn_u32 v48, v115, v114\n"
        "s_mov_b64 exec, s[82:83]\n"
        "s_waitcnt lgkmcnt(11)\n"
        "v_cmpx_le_u32_e32 vcc, s56, v49\n"
        "ds_add_rtn_u32 v49, v115, v114\n"
        "s_mov_b64 exec, -1\n"
        "s_nop 1\n"
        "s_add_u32 s52, s53, 272\n"
        "v_readlane_b32 s54, v107, 0\n"
        "v_readlane_b32 s55, v107, 1\n"
        "s_nop 0\n"
        "v_mbcnt_lo_u32_b32 v110, s54, 0\n"
        "v_mbcnt_hi_u32_b32 v110, s55, v110\n"
        "v_lshl_add_u32 v110, v110, 2, s52\n"
        "ds_read_b32 v102, v110\n"
        "s_bcnt1_i32_b64 s54, s[54:55]\n"
        "s_lshl2_add_u32 s52, s54, s52\n"
        "v_readlane_b32 s54, v107, 2\n"
        "v_readlane_b32 s55, v107, 3\n"
        "s_nop 0\n"
        "v_mbcnt_lo_u32_b32 v110, s54, 0\n"
        "v_mbcnt_hi_u32_b32 v110, s55, v110\n"
        "v_lshl_add_u32 v110, v110, 2, s52\n"
        "ds_read_b32 v103, v110\n"
        "s_bcnt1_i32_b64 s54, s[54:55]\n"
        "s_lshl2_add_u32 s52, s54, s52\n"
        "v_readlane_b32 s54, v107, 4\n"
        "v_readlane_b32 s55, v107, 5\n"
        "s_nop 0\n"
        "v_mbcnt_lo_u32_b32 v110, s54, 0\n"
        "v_mbcnt_hi_u32_b32 v110, s55, v110\n"
        "v_lshl_add_u32 v110, v110, 2, s52\n"
        "ds_read_b32 v104, v110\n"
        "s_bcnt1_i32_b64 s54, s[54:55]\n"
        "s_lshl2_add_u32 s52, s54, s52\n"
        "v_readlane_b32 s54, v107, 6\n"
        "v_readlane_b32 s55, v107, 7\n"
        "s_nop 0\n"
        "v_mbcnt_lo_u32_b32 v110, s54, 0\n"
        "v_mbcnt_hi_u32_b32 v110, s55, v110\n"
        "v_lshl_add_u32 v110, v110, 2, s52\n"
        "s_waitcnt lgkmcnt(14)\n"
        "ds_read_b32 v105, v110\n"
        "s_bcnt1_i32_b64 s54, s[54:55]\n"
        "s_lshl2_add_u32 s52, s54, s52\n"
        "v_readlane_b32 s54, v107, 8\n"
        "v_readlane_b32 s55, v107, 9\n"
        "s_nop 0\n"
        "v_mbcnt_lo_u32_b32 v110, s54, 0\n"
        "v_mbcnt_hi_u32_b32 v110, s55, v110\n"
        "v_lshl_add_u32 v110, v110, 2, s52\n"
        "s_waitcnt lgkmcnt(14)\n"
        "ds_read_b32 v106, v110\n"
        "s_bcnt1_i32_b64 s54, s[54:55]\n"
        "s_lshl2_add_u32 s52, s54, s52\n"
        "v_readlane_b32 s54, v107, 10\n"
        "v_readlane_b32 s55, v107, 11\n"
        "s_nop 0\n"
        "v_mbcnt_lo_u32_b32 v110, s54, 0\n"
        "v_mbcnt_hi_u32_b32 v110, s55, v110\n"
        "v_lshl_add_u32 v110, v110, 2, s52\n"
        "s_waitcnt lgkmcnt(14)\n"
        "ds_read_b32 v107, v110\n"
        "s_mov_b64 exec, s[60:61]\n"
        "v_and_b32 v38, 0x7ffff, v38\n"
        "s_waitcnt lgkmcnt(14)\n"
        "ds_write_b64 v38, v[14:15]\n"
        "s_mov_b64 exec, s[62:63]\n"
        "v_and_b32 v39, 0x7ffff, v39\n"
        "s_waitcnt lgkmcnt(14)\n"
        "ds_write_b64 v39, v[16:17]\n"
        "s_mov_b64 exec, -1\n"
        "s_waitcnt lgkmcnt(7)\n"
        "v_add_u32 v102, v6, v102\n"
        "s_waitcnt lgkmcnt(6)\n"
        "v_add_u32 v103, v6, v103\n"
        "s_waitcnt lgkmcnt(5)\n"
        "v_add_u32 v104, v6, v104\n"
        "s_waitcnt lgkmcnt(4)\n"
        "v_add_u32 v105, v6, v105\n"
        "s_waitcnt lgkmcnt(3)\n"
        "v_add_u32 v106, v6, v106\n"
        "s_waitcnt lgkmcnt(2)\n"
        "v_add_u32 v107, v6, v107\n"
        "s_mov_b64 exec, s[64:65]\n"
        "v_and_b32 v40, 0x7ffff, v40\n"
        "ds_write_b64 v40, v[18:19]\n"
        "s_mov_b64 exec, s[66:67]\n"
        "v_and_b32 v41, 0x7ffff, v41\n"
        "ds_write_b64 v41, v[20:21]\n"
        "s_mov_b64 exec, s[68:69]\n"
        "v_and_b32 v42, 0x7ffff, v42\n"
        "ds_write_b64 v42, v[22:23]\n"
        "s_mov_b64 exec, s[70:71]\n"
        "v_and_b32 v43, 0x7ffff, v43\n"
        "ds_write_b64 v43, v[24:25]\n"
        "s_mov_b64 exec, s[72:73]\n"
        "v_and_b32 v44, 0x7ffff, v44\n"
        "ds_write_b64 v44, v[26:27]\n"
        "s_mov_b64 exec, s[74:75]\n"
        "v_and_b32 v45, 0x7ffff, v45\n"
        "ds_write_b64 v45, v[28:29]\n"
        "s_mov_b64 exec, s[76:77]\n"
        "v_and_b32 v46, 0x7ffff, v46\n"
        "ds_write_b64 v46, v[30:31]\n"
        "s_mov_b64 exec, s[78:79]\n"
        "v_and_b32 v47, 0x7ffff, v47\n"
        "ds_write_b64 v47, v[32:33]\n"
        "s_mov_b64 exec, s[80:81]\n"
        "v_and_b32 v48, 0x7ffff, v48\n"
        "ds_write_b64 v48, v[34:35]\n"
        "s_mov_b64 exec, s[82:83]\n"
        "v_and_b32 v49, 0x7ffff, v49\n"
        "ds_write_b64 v49, v[36:37]\n"
        "s_mov_b64 exec, -1\n"
        "s_bitcmp1_b32 %[big], 0\n"
        "s_cbranch_scc0 .Lg1_%=\n"
        "s_waitcnt vmcnt(0) lgkmcnt(0)\n"
        "ds_read_b32 v111, v11 offset:0\n"
        "s_waitcnt lgkmcnt(0)\n"
        "v_readlane_b32 s54, v111, 12\n"
        "v_readlane_b32 s55, v111, 13\n"
        "s_nop 0\n"
        "v_mbcnt_lo_u32_b32 v110, s54, 0\n"
        "v_mbcnt_hi_u32_b32 v110, s55, v110\n"
        "s_bcnt1_i32_b64 s54, s[54:55]\n"
        "s_sub_u32 s54, 32, s54\n"
        "s_lshl2_add_u32 s52, s54, s53\n"
        "v_lshl_add_u32 v110, v110, 2, s52\n"
        "ds_read_b32 v110, v110 offset:64\n"
        "s_waitcnt lgkmcnt(0)\n"
        "v_add_u32 v110, v6, v110\n"
        "v_add_u32 v110, 3840, v110\n"
        "v_and_b32 v17, 2, v110\n"
        "v_and_b32 v110, -4, v110\n"
        "buffer_load_dwordx3 v[14:16], v110, s[36:39], 0 offen sc1\n"
        "v_cmp_ne_u32_e32 vcc, 0, v17\n"
        "s_waitcnt vmcnt(0)\n"
        "v_and_b32 v110, 0x7ffc0000, v15\n"
        "v_cmp_class_f32_e64 s[88:89], v15, %[cls]\n"
        "v_cndmask_b32_e32 v17, v14, v16, vcc\n"
        "v_mul_hi_u32 v110, v110, s47\n"
        "v_cndmask_b32_sdwa v16, v16, v14, vcc dst_sel:DWORD dst_unused:UNUSED_PAD src0_sel:WORD_0 src1_sel:WORD_1\n"
        "v_and_b32 v111, 0x7ffc0000, v17\n"
        "v_cmp_class_f32_e64 s[90:91], v17, %[cls]\n"
        "v_mul_hi_u32 v111, v111, s47\n"
        "v_and_b32 v110, -4, v110\n"
        "v_and_b32 v111, -4, v111\n"
        "s_mov_b64 exec, s[88:89]\n"
        "ds_add_rtn_u32 v112, v110, v114 offset:20016\n"
        "s_mov_b64 exec, s[90:91]\n"
        "ds_add_rtn_u32 v113, v111, v114 offset:20016\n"
        "s_mov_b64 exec, -1\n"
        "v_perm_b32 v14, v15, v16, s92\n"
        "v_perm_b32 v16, v17, v16, s93\n"
        "v_and_b32 v14, 0x7ff0000, v14\n"
        "v_and_b32 v16, 0x7ff0000, v16\n"
        "s_mov_b64 exec, s[88:89]\n"
        "s_waitcnt lgkmcnt(1)\n"
        "v_cmpx_le_u32_e32 vcc, s56, v112\n"
        "ds_add_rtn_u32 v112, v115, v114\n"
        "s_mov_b64 exec, s[90:91]\n"
        "s_waitcnt lgkmcnt(1)\n"
        "v_cmpx_le_u32_e32 vcc, s56, v113\n"
        "ds_add_rtn_u32 v113, v115, v114\n"
        "s_mov_b64 exec, -1\n"
        "s_nop 1\n"
        "s_mov_b64 exec, s[88:89]\n"
        "s_waitcnt lgkmcnt(1)\n"
        "v_and_b32 v112, 0x7ffff, v112\n"
        "ds_write_b64 v112, v[14:15]\n"
        "s_mov_b64 exec, s[90:91]\n"
        "s_waitcnt lgkmcnt(1)\n"
        "v_and_b32 v113, 0x7ffff, v113\n"
        "ds_write_b64 v113, v[16:17]\n"
        "s_mov_b64 exec, -1\n"
        "s_waitcnt lgkmcnt(0)\n"
        ".Lg1_%=:\n"
        "s_nop 1\n"
        "v_and_b32 v17, 2, v102\n"
        "v_and_b32 v102, -4, v102\n"
        "buffer_load_dwordx3 v[14:16], v102, s[36:39], 0 offen offset:0 sc1\n"
        "v_and_b32 v21, 2, v103\n"
        "v_and_b32 v103, -4, v103\n"
        "buffer_load_dwordx3 v[18:20], v103, s[36:39], 0 offen offset:640 sc1\n"
        "v_and_b32 v25, 2, v104\n"
        "v_and_b32 v104, -4, v104\n"
        "buffer_load_dwordx3 v[22:24], v104, s[36:39], 0 offen offset:1280 sc1\n"
        "v_and_b32 v29, 2, v105\n"
        "v_and_b32 v105, -4, v105\n"
        "buffer_load_dwordx3 v[26:28], v105, s[36:39], 0 offen offset:1920 sc1\n"
        "v_and_b32 v33, 2, v106\n"
        "v_and_b32 v106, -4, v106\n"
        "buffer_load_dwordx3 v[30:32], v106, s[36:39], 0 offen offset:2560 sc1\n"
        "v_and_b32 v37, 2, v107\n"
        "v_and_b32 v107, -4, v107\n"
        "buffer_load_dwordx3 v[34:36], v107, s[36:39], 0 offen offset:3200 sc1\n"
        "s_waitcnt lgkmcnt(0)\n"
        "s_barrier\n"
        "s_setprio 3\n"
        "v_mbcnt_lo_u32_b32 v111, -1, 0\n"
        "v_mbcnt_hi_u32_b32 v111, -1, v111\n"
        "v_lshlrev_b32 v110, 4, v111\n"
        "v_and_b32 v111, 3, v111\n"
        "v_lshlrev_b32 v111, 2, v111\n"
        "ds_read_b32 v76, v111 offset:20800\n"
        "ds_read_b128 v[92:95], v110 offset:20864\n"
        "v_cmp_eq_u32_e32 vcc, 0, v111\n"
        "v_lshlrev_b32 v111, 2, v111\n"
        "v_mov_b32 v77, 93\n"
        "ds_read_b128 v[72:75], v111 offset:21888\n"
        "v_cndmask_b32_e64 v77, v77, 63, vcc\n"
        "ds_read_b32 v110, v8\n"
        "ds_read_b64 v[38:39], v7 offset:0\n"
        "ds_read_b64 v[40:41], v7 offset:520\n"
        "ds_read_b64 v[42:43], v7 offset:1040\n"
        "ds_read_b64 v[44:45], v7 offset:1560\n"
        "ds_read_b64 v[46:47], v7 offset:2080\n"
        "ds_read_b64 v[48:49], v7 offset:2600\n"
        "ds_read_b64 v[50:51], v7 offset:3120\n"
        "ds_read_b64 v[52:53], v7 offset:3640\n"
        "ds_read_b64 v[54:55], v7 offset:4160\n"
        "v_subrev_u32 v111, %[ovd], v7\n"
        "ds_read_b64 v[56:57], v111 offset:4680\n"
        "v_subrev_u32 v111, %[ovd], v111\n"
        "ds_read_b64 v[58:59], v111 offset:5200\n"
        "v_subrev_u32 v111, %[ovd], v111\n"
        "s_waitcnt lgkmcnt(14)\n"
        "ds_read_b64 v[60:61], v111 offset:5720\n"
        "s_waitcnt lgkmcnt(14)\n"
        "ds_write_b32 v8, v9\n"
        "s_waitcnt lgkmcnt(13)\n"
        "v_and_b32 v110, 0xffffff, v110\n"
        "v_sub_u32 v110, v110, v10\n"
        "v_ashrrev_i32 v110, 3, v110\n"
        "v_add_u32 v12, v12, v110\n"
        "v_max_i32 v13, v13, v110\n"
        "v_med3_i32 v110, v110, 0, 63\n"
        "s_nop 0\n"
        "v_readlane_b32 s60, v110, 0\n"
        "v_readlane_b32 s62, v110, 1\n"
        "v_readlane_b32 s64, v110, 2\n"
        "v_readlane_b32 s66, v110, 3\n"
        "v_readlane_b32 s68, v110, 4\n"
        "v_readlane_b32 s70, v110, 5\n"
        "v_readlane_b32 s72, v110, 6\n"
        "v_readlane_b32 s74, v110, 7\n"
        "v_readlane_b32 s76, v110, 8\n"
        "v_readlane_b32 s78, v110, 9\n"
        "v_readlane_b32 s80, v110, 10\n"
        "v_readlane_b32 s82, v110, 11\n"
        "s_bfm_b64 s[60:61], s60, 0\n"
        "s_bfm_b64 s[62:63], s62, 0\n"
        "s_bfm_b64 s[64:65], s64, 0\n"
        "s_bfm_b64 s[66:67], s66, 0\n"
        "s_bfm_b64 s[68:69], s68, 0\n"
        "s_bfm_b64 s[70:71], s70, 0\n"
        "s_bfm_b64 s[72:73], s72, 0\n"
        "s_bfm_b64 s[74:75], s74, 0\n"
        "s_bfm_b64 s[76:77], s76, 0\n"
        "s_bfm_b64 s[78:79], s78, 0\n"
        "s_bfm_b64 s[80:81], s80, 0\n"
        "s_bfm_b64 s[82:83], s82, 0\n"
        "v_lshrrev_b32 v76, 23, v76\n"
        "v_max_u32 v76, 64, v76\n"
        "v_sub_u32 v76, v77, v76\n"
        "v_pk_mad_u16 v96, v74, v76, v72 op_sel_hi:[1,0,1]\n"
        "v_pk_mad_u16 v97, v75, v76, v73 op_sel_hi:[1,0,1]\n"
        "s_waitcnt lgkmcnt(12)\n"
        "v_bfe_u32 v101, v39, 14, 17\n"
        "s_mov_b64 exec, s[60:61]\n"
        "buffer_load_dwordx2 v[66:67], v101, s[40:43], 0 idxen\n"
        "s_mov_b64 exec, -1\n"
        "s_waitcnt lgkmcnt(0)\n"
        "s_barrier\n"
        "v_bfe_u32 v101, v41, 14, 17\n"
        "s_mov_b64 exec, s[62:63]\n"
        "buffer_load_dwordx2 v[68:69], v101, s[40:43], 0 idxen\n"
        "s_mov_b64 exec, s[60:61]\n"
        "v_and_b32 v108, 0x3ff8, v39\n"
        "v_add_u32 v111, v108, v108\n"
        "ds_read_b128 v[88:91], v111 offset:0\n"
        "s_mov_b64 exec, -1\n"
        "v_cmp_ne_u32_e32 vcc, 0, v17\n"
        "s_waitcnt vmcnt(7)\n"
        "v_and_b32 v110, 0x7ffc0000, v15\n"
        "v_cmp_class_f32_e64 s[88:89], v15, %[cls]\n"
        "v_cndmask_b32_e32 v17, v14, v16, vcc\n"
        "v_mul_hi_u32 v110, v110, s47\n"
        "v_cndmask_b32_sdwa v16, v16, v14, vcc dst_sel:DWORD dst_unused:UNUSED_PAD src0_sel:WORD_0 src1_sel:WORD_1\n"
        "v_and_b32 v111, 0x7ffc0000, v17\n"
        "v_cmp_class_f32_e64 s[90:91], v17, %[cls]\n"
        "v_mul_hi_u32 v111, v111, s47\n"
        "v_and_b32 v110, -4, v110\n"
        "v_and_b32 v111, -4, v111\n"
        "s_mov_b64 exec, s[88:89]\n"
        "ds_add_rtn_u32 v112, v110, v114 offset:20016\n"
        "s_mov_b64 exec, s[90:91]\n"
        "ds_add_rtn_u32 v113, v111, v114 offset:20016\n"
        "s_mov_b64 exec, -1\n"
        "v_perm_b32 v14, v15, v16, s92\n"
        "v_perm_b32 v16, v17, v16, s93\n"
        "v_and_b32 v14, 0x7ff0000, v14\n"
        "v_and_b32 v16, 0x7ff0000, v16\n"
        "v_bfe_u32 v101, v43, 14, 17\n"
        "v_pk_mul_f32 v[98:99], v[38:39], s[44:45] op_sel_hi:[0,1]\n"
        "v_mul_f32 v100, s46, v38\n"
        "s_waitcnt lgkmcnt(2)\n"
        "v_mfma_f32_32x32x16_bf16 v[72:87], v[92:95], v[88:91], 0\n"
        "v_cos_f32 v98, v98\n"
        "v_cos_f32 v99, v99\n"
        "v_cos_f32 v100, v100\n"
        "s_mov_b64 exec, s[62:63]\n"
        "v_and_b32 v109, 0x3ff8, v41\n"
        "v_add_u32 v111, v109, v109\n"
        "ds_read_b128 v[88:91], v111 offset:0\n"
        "s_mov_b64 exec, s[64:65]\n"
        "buffer_load_dwordx2 v[70:71], v101, s[40:43], 0 idxen\n"
        "s_mov_b64 exec, -1\n"
        "s_waitcnt vmcnt(2)\n"
        "s_nop 0\n"
        "v_mfma_f32_4x4x4_16b_bf16 v[84:87], v[96:97], v[66:67], 0\n"
        "s_mov_b64 exec, s[60:61]\n"
        "v_pk_mul_f32 v[72:73], v[72:73], v[98:99] op_sel_hi:[1,0]\n"
        "v_pk_mul_f32 v[74:75], v[74:75], v[98:99] op_sel_hi:[1,0]\n"
        "v_pk_fma_f32 v[72:73], v[76:77], v[98:99], v[72:73] op_sel:[0,1,0] op_sel_hi:[1,1,1]\n"
        "v_pk_fma_f32 v[74:75], v[78:79], v[98:99], v[74:75] op_sel:[0,1,0] op_sel_hi:[1,1,1]\n"
        "v_pk_fma_f32 v[72:73], v[80:81], v[100:101], v[72:73] op_sel_hi:[1,0,1]\n"
        "v_pk_fma_f32 v[74:75], v[82:83], v[100:101], v[74:75] op_sel_hi:[1,0,1]\n"
        "v_pk_fma_f32 v[72:73], v[84:85], v[72:73], s[48:49]\n"
        "v_pk_fma_f32 v[74:75], v[86:87], v[74:75], s[50:51]\n"
        "ds_add_u64 v108, v[72:73] offset:22072\n"
        "ds_add_u64 v108, v[74:75] offset:32072\n"
        "s_mov_b64 exec, s[88:89]\n"
        "s_waitcnt lgkmcnt(4)\n"
        "v_cmp_le_u32_e32 vcc, s56, v112\n"
        "s_cbranch_vccz .Lg2_%=\n"
        "s_mov_b64 exec, vcc\n"
        "ds_add_rtn_u32 v112, v115, v114\n"
        "s_mov_b64 exec, s[88:89]\n"
        "s_waitcnt lgkmcnt(0)\n"
        ".Lg2_%=:\n"
        "v_and_b32 v112, 0x7ffff, v112\n"
        "ds_write_b64 v112, v[14:15]\n"
        "s_mov_b64 exec, s[90:91]\n"
        "s_waitcnt lgkmcnt(4)\n"
        "v_cmp_le_u32_e32 vcc, s56, v113\n"
        "s_cbranch_vccz .Lg3_%=\n"
        "s_mov_b64 exec, vcc\n"
        "ds_add_rtn_u32 v113, v115, v114\n"
        "s_mov_b64 exec, s[90:91]\n"
        "s_waitcnt lgkmcnt(0)\n"
        ".Lg3_%=:\n"
        "v_and_b32 v113, 0x7ffff, v113\n"
        "ds_write_b64 v113, v[16:17]\n"
        "s_mov_b64 exec, -1\n"
        "s_setprio 2\n"
        "v_bfe_u32 v101, v45, 14, 17\n"
        "v_pk_mul_f32 v[98:99], v[40:41], s[44:45] op_sel_hi:[0,1]\n"
        "v_mul_f32 v100, s46, v40\n"
        "s_waitcnt lgkmcnt(4)\n"
        "v_mfma_f32_32x32x16_bf16 v[72:87], v[92:95], v[88:91], 0\n"
        "v_cos_f32 v98, v98\n"
        "v_cos_f32 v99, v99\n"
        "v_cos_f32 v100, v100\n"
        "s_mov_b64 exec, s[64:65]\n"
        "v_and_b32 v108, 0x3ff8, v43\n"
        "v_add_u32 v111, v108, v108\n"
        "ds_read_b128 v[88:91], v111 offset:0\n"
        "s_mov_b64 exec, s[66:67]\n"
        "buffer_load_dwordx2 v[66:67], v101, s[40:43], 0 idxen\n"
        "s_mov_b64 exec, -1\n"
        "s_waitcnt vmcnt(2)\n"
        "s_nop 0\n"
        "v_mfma_f32_4x4x4_16b_bf16 v[84:87], v[96:97], v[68:69], 0\n"
        "s_mov_b64 exec, s[62:63]\n"
        "v_pk_mul_f32 v[72:73], v[72:73], v[98:99] op_sel_hi:[1,0]\n"
        "v_pk_mul_f32 v[74:75], v[74:75], v[98:99] op_sel_hi:[1,0]\n"
        "v_pk_fma_f32 v[72:73], v[76:77], v[98:99], v[72:73] op_sel:[0,1,0] op_sel_hi:[1,1,1]\n"
        "v_pk_fma_f32 v[74:75], v[78:79], v[98:99], v[74:75] op_sel:[0,1,0] op_sel_hi:[1,1,1]\n"
        "v_pk_fma_f32 v[72:73], v[80:81], v[100:101], v[72:73] op_sel_hi:[1,0,1]\n"
        "v_pk_fma_f32 v[74:75], v[82:83], v[100:101], v[74:75] op_sel_hi:[1,0,1]\n"
        "v_pk_fma_f32 v[72:73], v[84:85], v[72:73], s[48:49]\n"
        "v_pk_fma_f32 v[74:75], v[86:87], v[74:75], s[50:51]\n"
        "ds_add_u64 v109, v[72:73] offset:22072\n"
        "ds_add_u64 v109, v[74:75] offset:32072\n"
        "s_mov_b64 exec, -1\n"
        "v_cmp_ne_u32_e32 vcc, 0, v21\n"
        "v_and_b32 v110, 0x7ffc0000, v19\n"
        "v_cmp_class_f32_e64 s[88:89], v19, %[cls]\n"
        "v_cndmask_b32_e32 v21, v18, v20, vcc\n"
        "v_mul_hi_u32 v110, v110, s47\n"
        "v_cndmask_b32_sdwa v20, v20, v18, vcc dst_sel:DWORD dst_unused:UNUSED_PAD src0_sel:WORD_0 src1_sel:WORD_1\n"
        "v_and_b32 v111, 0x7ffc0000, v21\n"
        "v_cmp_class_f32_e64 s[90:91], v21, %[cls]\n"
        "v_mul_hi_u32 v111, v111, s47\n"
        "v_and_b32 v110, -4, v110\n"
        "v_and_b32 v111, -4, v111\n"
        "s_mov_b64 exec, s[88:89]\n"
        "ds_add_rtn_u32 v112, v110, v114 offset:20016\n"
        "s_mov_b64 exec, s[90:91]\n"
        "ds_add_rtn_u32 v113, v111, v114 offset:20016\n"
        "s_mov_b64 exec, -1\n"
        "v_perm_b32 v18, v19, v20, s92\n"
        "v_perm_b32 v20, v21, v20, s93\n"
        "v_and_b32 v18, 0x7ff0000, v18\n"
        "v_and_b32 v20, 0x7ff0000, v20\n"
        "v_bfe_u32 v101, v47, 14, 17\n"
        "v_pk_mul_f32 v[98:99], v[42:43], s[44:45] op_sel_hi:[0,1]\n"
        "v_mul_f32 v100, s46, v42\n"
        "s_waitcnt lgkmcnt(4)\n"
        "v_mfma_f32_32x32x16_bf16 v[72:87], v[92:95], v[88:91], 0\n"
        "v_cos_f32 v98, v98\n"
        "v_cos_f32 v99, v99\n"
        "v_cos_f32 v100, v100\n"
        "s_mov_b64 exec, s[66:67]\n"
        "v_and_b32 v109, 0x3ff8, v45\n"
        "v_add_u32 v111, v109, v109\n"
        "ds_read_b128 v[88:91], v111 offset:0\n"
        "s_mov_b64 exec, s[68:69]\n"
        "buffer_load_dwordx2 v[68:69], v101, s[40:43], 0 idxen\n"
        "s_mov_b64 exec, -1\n"
        "s_waitcnt vmcnt(2)\n"
        "s_nop 0\n"
        "v_mfma_f32_4x4x4_16b_bf16 v[84:87], v[96:97], v[70:71], 0\n"
        "s_mov_b64 exec, s[64:65]\n"
        "v_pk_mul_f32 v[72:73], v[72:73], v[98:99] op_sel_hi:[1,0]\n"
        "v_pk_mul_f32 v[74:75], v[74:75], v[98:99] op_sel_hi:[1,0]\n"
        "v_pk_fma_f32 v[72:73], v[76:77], v[98:99], v[72:73] op_sel:[0,1,0] op_sel_hi:[1,1,1]\n"
        "v_pk_fma_f32 v[74:75], v[78:79], v[98:99], v[74:75] op_sel:[0,1,0] op_sel_hi:[1,1,1]\n"
        "v_pk_fma_f32 v[72:73], v[80:81], v[100:101], v[72:73] op_sel_hi:[1,0,1]\n"
        "v_pk_fma_f32 v[74:75], v[82:83], v[100:101], v[74:75] op_sel_hi:[1,0,1]\n"
        "v_pk_fma_f32 v[72:73], v[84:85], v[72:73], s[48:49]\n"
        "v_pk_fma_f32 v[74:75], v[86:87], v[74:75], s[50:51]\n"
        "ds_add_u64 v108, v[72:73] offset:22072\n"
        "ds_add_u64 v108, v[74:75] offset:32072\n"
        "s_mov_b64 exec, s[88:89]\n"
        "s_waitcnt lgkmcnt(4)\n"
        "v_cmp_le_u32_e32 vcc, s56, v112\n"
        "s_cbranch_vccz .Lg4_%=\n"
        "s_mov_b64 exec, vcc\n"
        "ds_add_rtn_u32 v112, v115, v114\n"
        "s_mov_b64 exec, s[88:89]\n"
        "s_waitcnt lgkmcnt(0)\n"
        ".Lg4_%=:\n"
        "v_and_b32 v112, 0x7ffff, v112\n"
        "ds_write_b64 v112, v[18:19]\n"
        "s_mov_b64 exec, s[90:91]\n"
        "s_waitcnt lgkmcnt(4)\n"
        "v_cmp_le_u32_e32 vcc, s56, v113\n"
        "s_cbranch_vccz .Lg5_%=\n"
        "s_mov_b64 exec, vcc\n"
        "ds_add_rtn_u32 v113, v115, v114\n"
        "s_mov_b64 exec, s[90:91]\n"
        "s_waitcnt lgkmcnt(0)\n"
        ".Lg5_%=:\n"
        "v_and_b32 v113, 0x7ffff, v113\n"
        "ds_write_b64 v113, v[20:21]\n"
        "s_mov_b64 exec, -1\n"
        "ds_read_b32 v107, v11 offset:416\n"
        "v_bfe_u32 v101, v49, 14, 17\n"
        "v_pk_mul_f32 v[98:99], v[44:45], s[44:45] op_sel_hi:[0,1]\n"
        "v_mul_f32 v100, s46, v44\n"
        "s_waitcnt lgkmcnt(5)\n"
        "v_mfma_f32_32x32x16_bf16 v[72:87], v[92:95], v[88:91], 0\n"
        "v_cos_f32 v98, v98\n"
        "v_cos_f32 v99, v99\n"
        "v_cos_f32 v100, v100\n"
        "s_mov_b64 exec, s[68:69]\n"
        "v_and_b32 v108, 0x3ff8, v47\n"
        "v_add_u32 v111, v108, v108\n"
        "ds_read_b128 v[88:91], v111 offset:0\n"
        "s_mov_b64 exec, s[70:71]\n"
        "buffer_load_dwordx2 v[70:71], v101, s[40:43], 0 idxen\n"
        "s_mov_b64 exec, -1\n"
        "s_waitcnt vmcnt(2)\n"
        "s_nop 0\n"
        "v_mfma_f32_4x4x4_16b_bf16 v[84:87], v[96:97], v[66:67], 0\n"
        "s_mov_b64 exec, s[66:67]\n"
        "v_pk_mul_f32 v[72:73], v[72:73], v[98:99] op_sel_hi:[1,0]\n"
        "v_pk_mul_f32 v[74:75], v[74:75], v[98:99] op_sel_hi:[1,0]\n"
        "v_pk_fma_f32 v[72:73], v[76:77], v[98:99], v[72:73] op_sel:[0,1,0] op_sel_hi:[1,1,1]\n"
        "v_pk_fma_f32 v[74:75], v[78:79], v[98:99], v[74:75] op_sel:[0,1,0] op_sel_hi:[1,1,1]\n"
        "v_pk_fma_f32 v[72:73], v[80:81], v[100:101], v[72:73] op_sel_hi:[1,0,1]\n"
        "v_pk_fma_f32 v[74:75], v[82:83], v[100:101], v[74:75] op_sel_hi:[1,0,1]\n"
        "v_pk_fma_f32 v[72:73], v[84:85], v[72:73], s[48:49]\n"
        "v_pk_fma_f32 v[74:75], v[86:87], v[74:75], s[50:51]\n"
        "ds_add_u64 v109, v[72:73] offset:22072\n"
        "ds_add_u64 v109, v[74:75] offset:32072\n"
        "s_mov_b64 exec, -1\n"
        "v_cmp_ne_u32_e32 vcc, 0, v25\n"
        "v_and_b32 v110, 0x7ffc0000, v23\n"
        "v_cmp_class_f32_e64 s[88:89], v23, %[cls]\n"
        "v_cndmask_b32_e32 v25, v22, v24, vcc\n"
        "v_mul_hi_u32 v110, v110, s47\n"
        "v_cndmask_b32_sdwa v24, v24, v22, vcc dst_sel:DWORD dst_unused:UNUSED_PAD src0_sel:WORD_0 src1_sel:WORD_1\n"
        "v_and_b32 v111, 0x7ffc0000, v25\n"
        "v_cmp_class_f32_e64 s[90:91], v25, %[cls]\n"
        "v_mul_hi_u32 v111, v111, s47\n"
        "v_and_b32 v110, -4, v110\n"
        "v_and_b32 v111, -4, v111\n"
        "s_mov_b64 exec, s[88:89]\n"
        "ds_add_rtn_u32 v112, v110, v114 offset:20016\n"
        "s_mov_b64 exec, s[90:91]\n"
        "ds_add_rtn_u32 v113, v111, v114 offset:20016\n"
        "s_mov_b64 exec, -1\n"
        "v_perm_b32 v22, v23, v24, s92\n"
        "v_perm_b32 v24, v25, v24, s93\n"
        "v_and_b32 v22, 0x7ff0000, v22\n"
        "v_and_b32 v24, 0x7ff0000, v24\n"
        "s_setprio 1\n"
        "v_bfe_u32 v101, v51, 14, 17\n"
        "v_pk_mul_f32 v[98:99], v[46:47], s[44:45] op_sel_hi:[0,1]\n"
        "v_mul_f32 v100, s46, v46\n"
        "s_waitcnt lgkmcnt(4)\n"
        "v_mfma_f32_32x32x16_bf16 v[72:87], v[92:95], v[88:91], 0\n"
        "v_cos_f32 v98, v98\n"
        "v_cos_f32 v99, v99\n"
        "v_cos_f32 v100, v100\n"
        "s_mov_b64 exec, s[70:71]\n"
        "v_and_b32 v109, 0x3ff8, v49\n"
        "v_add_u32 v111, v109, v109\n"
        "ds_read_b128 v[88:91], v111 offset:0\n"
        "s_mov_b64 exec, s[72:73]\n"
        "buffer_load_dwordx2 v[66:67], v101, s[40:43], 0 idxen\n"
        "s_mov_b64 exec, -1\n"
        "s_waitcnt vmcnt(2)\n"
        "s_nop 0\n"
        "v_mfma_f32_4x4x4_16b_bf16 v[84:87], v[96:97], v[68:69], 0\n"
        "s_mov_b64 exec, s[68:69]\n"
        "v_pk_mul_f32 v[72:73], v[72:73], v[98:99] op_sel_hi:[1,0]\n"
        "v_pk_mul_f32 v[74:75], v[74:75], v[98:99] op_sel_hi:[1,0]\n"
        "v_pk_fma_f32 v[72:73], v[76:77], v[98:99], v[72:73] op_sel:[0,1,0] op_sel_hi:[1,1,1]\n"
        "v_pk_fma_f32 v[74:75], v[78:79], v[98:99], v[74:75] op_sel:[0,1,0] op_sel_hi:[1,1,1]\n"
        "v_pk_fma_f32 v[72:73], v[80:81], v[100:101], v[72:73] op_sel_hi:[1,0,1]\n"
        "v_pk_fma_f32 v[74:75], v[82:83], v[100:101], v[74:75] op_sel_hi:[1,0,1]\n"
        "v_pk_fma_f32 v[72:73], v[84:85], v[72:73], s[48:49]\n"
        "v_pk_fma_f32 v[74:75], v[86:87], v[74:75], s[50:51]\n"
        "ds_add_u64 v108, v[72:73] offset:22072\n"
        "ds_add_u64 v108, v[74:75] offset:32072\n"
        "s_mov_b64 exec, s[88:89]\n"
        "s_waitcnt lgkmcnt(4)\n"
        "v_cmp_le_u32_e32 vcc, s56, v112\n"
        "s_cbranch_vccz .Lg6_%=\n"
        "s_mov_b64 exec, vcc\n"
        "ds_add_rtn_u32 v112, v115, v114\n"
        "s_mov_b64 exec, s[88:89]\n"
        "s_waitcnt lgkmcnt(0)\n"
        ".Lg6_%=:\n"
        "v_and_b32 v112, 0x7ffff, v112\n"
        "ds_write_b64 v112, v[22:23]\n"
        "s_mov_b64 exec, s[90:91]\n"
        "s_waitcnt lgkmcnt(4)\n"
        "v_cmp_le_u32_e32 vcc, s56, v113\n"
        "s_cbranch_vccz .Lg7_%=\n"
        "s_mov_b64 exec, vcc\n"
        "ds_add_rtn_u32 v113, v115, v114\n"
        "s_mov_b64 exec, s[90:91]\n"
        "s_waitcnt lgkmcnt(0)\n"
        ".Lg7_%=:\n"
        "v_and_b32 v113, 0x7ffff, v113\n"
        "ds_write_b64 v113, v[24:25]\n"
        "s_mov_b64 exec, -1\n"
        "s_add_u32 s52, s53, 480\n"
        "v_readlane_b32 s54, v107, 0\n"
        "v_readlane_b32 s55, v107, 1\n"
        "s_nop 0\n"
        "v_mbcnt_lo_u32_b32 v110, s54, 0\n"
        "v_mbcnt_hi_u32_b32 v110, s55, v110\n"
        "v_lshl_add_u32 v110, v110, 2, s52\n"
        "ds_read_b32 v102, v110\n"
        "s_bcnt1_i32_b64 s54, s[54:55]\n"
        "s_lshl2_add_u32 s52, s54, s52\n"
        "v_readlane_b32 s54, v107, 2\n"
        "v_readlane_b32 s55, v107, 3\n"
        "s_nop 0\n"
        "v_mbcnt_lo_u32_b32 v110, s54, 0\n"
        "v_mbcnt_hi_u32_b32 v110, s55, v110\n"
        "v_lshl_add_u32 v110, v110, 2, s52\n"
        "ds_read_b32 v103, v110\n"
        "s_bcnt1_i32_b64 s54, s[54:55]\n"
        "s_lshl2_add_u32 s52, s54, s52\n"
        "v_readlane_b32 s54, v107, 4\n"
        "v_readlane_b32 s55, v107, 5\n"
        "s_nop 0\n"
        "v_mbcnt_lo_u32_b32 v110, s54, 0\n"
        "v_mbcnt_hi_u32_b32 v110, s55, v110\n"
        "v_lshl_add_u32 v110, v110, 2, s52\n"
        "ds_read_b32 v104, v110\n"
        "s_bcnt1_i32_b64 s54, s[54:55]\n"
        "s_lshl2_add_u32 s52, s54, s52\n"
        "v_readlane_b32 s54, v107, 6\n"
        "v_readlane_b32 s55, v107, 7\n"
        "s_nop 0\n"
        "v_mbcnt_lo_u32_b32 v110, s54, 0\n"
        "v_mbcnt_hi_u32_b32 v110, s55, v110\n"
        "v_lshl_add_u32 v110, v110, 2, s52\n"
        "ds_read_b32 v105, v110\n"
        "s_bcnt1_i32_b64 s54, s[54:55]\n"
        "s_lshl2_add_u32 s52, s54, s52\n"
        "v_readlane_b32 s54, v107, 8\n"
        "v_readlane_b32 s55, v107, 9\n"
        "s_nop 0\n"
        "v_mbcnt_lo_u32_b32 v110, s54, 0\n"
        "v_mbcnt_hi_u32_b32 v110, s55, v110\n"
        "v_lshl_add_u32 v110, v110, 2, s52\n"
        "ds_read_b32 v106, v110\n"
        "s_bcnt1_i32_b64 s54, s[54:55]\n"
        "s_lshl2_add_u32 s52, s54, s52\n"
        "v_readlane_b32 s54, v107, 10\n"
        "v_readlane_b32 s55, v107, 11\n"
        "s_nop 0\n"
        "v_mbcnt_lo_u32_b32 v110, s54, 0\n"
        "v_mbcnt_hi_u32_b32 v110, s55, v110\n"
        "v_lshl_add_u32 v110, v110, 2, s52\n"
        "ds_read_b32 v107, v110\n"
        "v_bfe_u32 v101, v53, 14, 17\n"
        "v_pk_mul_f32 v[98:99], v[48:49], s[44:45] op_sel_hi:[0,1]\n"
        "v_mul_f32 v100, s46, v48\n"
        "s_waitcnt lgkmcnt(10)\n"
        "v_mfma_f32_32x32x16_bf16 v[72:87], v[92:95], v[88:91], 0\n"
        "v_cos_f32 v98, v98\n"
        "v_cos_f32 v99, v99\n"
        "v_cos_f32 v100, v100\n"
        "s_mov_b64 exec, s[72:73]\n"
        "v_and_b32 v108, 0x3ff8, v51\n"
        "v_add_u32 v111, v108, v108\n"
        "ds_read_b128 v[88:91], v111 offset:0\n"
        "s_mov_b64 exec, s[74:75]\n"
        "buffer_load_dwordx2 v[68:69], v101, s[40:43], 0 idxen\n"
        "s_mov_b64 exec, -1\n"
        "s_waitcnt vmcnt(2)\n"
        "s_nop 0\n"
        "v_mfma_f32_4x4x4_16b_bf16 v[84:87], v[96:97], v[70:71], 0\n"
        "s_mov_b64 exec, s[70:71]\n"
        "v_pk_mul_f32 v[72:73], v[72:73], v[98:99] op_sel_hi:[1,0]\n"
        "v_pk_mul_f32 v[74:75], v[74:75], v[98:99] op_sel_hi:[1,0]\n"
        "v_pk_fma_f32 v[72:73], v[76:77], v[98:99], v[72:73] op_sel:[0,1,0] op_sel_hi:[1,1,1]\n"
        "v_pk_fma_f32 v[74:75], v[78:79], v[98:99], v[74:75] op_sel:[0,1,0] op_sel_hi:[1,1,1]\n"
        "v_pk_fma_f32 v[72:73], v[80:81], v[100:101], v[72:73] op_sel_hi:[1,0,1]\n"
        "v_pk_fma_f32 v[74:75], v[82:83], v[100:101], v[74:75] op_sel_hi:[1,0,1]\n"
        "v_pk_fma_f32 v[72:73], v[84:85], v[72:73], s[48:49]\n"
        "v_pk_fma_f32 v[74:75], v[86:87], v[74:75], s[50:51]\n"
        "ds_add_u64 v109, v[72:73] offset:22072\n"
        "ds_add_u64 v109, v[74:75] offset:32072\n"
        "s_mov_b64 exec, -1\n"
        "v_cmp_ne_u32_e32 vcc, 0, v29\n"
        "v_and_b32 v110, 0x7ffc0000, v27\n"
        "v_cmp_class_f32_e64 s[88:89], v27, %[cls]\n"
        "v_cndmask_b32_e32 v29, v26, v28, vcc\n"
        "v_mul_hi_u32 v110, v110, s47\n"
        "v_cndmask_b32_sdwa v28, v28, v26, vcc dst_sel:DWORD dst_unused:UNUSED_PAD src0_sel:WORD_0 src1_sel:WORD_1\n"
        "v_and_b32 v111, 0x7ffc0000, v29\n"
        "v_cmp_class_f32_e64 s[90:91], v29, %[cls]\n"
        "v_mul_hi_u32 v111, v111, s47\n"
        "v_and_b32 v110, -4, v110\n"
        "v_and_b32 v111, -4, v111\n"
        "s_mov_b64 exec, s[88:89]\n"
        "ds_add_rtn_u32 v112, v110, v114 offset:20016\n"
        "s_mov_b64 exec, s[90:91]\n"
        "ds_add_rtn_u32 v113, v111, v114 offset:20016\n"
        "s_mov_b64 exec, -1\n"
        "v_perm_b32 v26, v27, v28, s92\n"
        "v_perm_b32 v28, v29, v28, s93\n"
        "v_and_b32 v26, 0x7ff0000, v26\n"
        "v_and_b32 v28, 0x7ff0000, v28\n"
        "v_bfe_u32 v101, v55, 14, 17\n"
        "v_pk_mul_f32 v[98:99], v[50:51], s[44:45] op_sel_hi:[0,1]\n"
        "v_mul_f32 v100, s46, v50\n"
        "s_waitcnt lgkmcnt(4)\n"
        "v_mfma_f32_32x32x16_bf16 v[72:87], v[92:95], v[88:91], 0\n"
        "v_cos_f32 v98, v98\n"
        "v_cos_f32 v99, v99\n"
        "v_cos_f32 v100, v100\n"
        "s_mov_b64 exec, s[74:75]\n"
        "v_and_b32 v109, 0x3ff8, v53\n"
        "v_add_u32 v111, v109, v109\n"
        "ds_read_b128 v[88:91], v111 offset:0\n"
        "s_mov_b64 exec, s[76:77]\n"
        "buffer_load_dwordx2 v[70:71], v101, s[40:43], 0 idxen\n"
        "s_mov_b64 exec, -1\n"
        "s_waitcnt vmcnt(2)\n"
        "s_nop 0\n"
        "v_mfma_f32_4x4x4_16b_bf16 v[84:87], v[96:97], v[66:67], 0\n"
        "s_mov_b64 exec, s[72:73]\n"
        "v_pk_mul_f32 v[72:73], v[72:73], v[98:99] op_sel_hi:[1,0]\n"
        "v_pk_mul_f32 v[74:75], v[74:75], v[98:99] op_sel_hi:[1,0]\n"
        "v_pk_fma_f32 v[72:73], v[76:77], v[98:99], v[72:73] op_sel:[0,1,0] op_sel_hi:[1,1,1]\n"
        "v_pk_fma_f32 v[74:75], v[78:79], v[98:99], v[74:75] op_sel:[0,1,0] op_sel_hi:[1,1,1]\n"
        "v_pk_fma_f32 v[72:73], v[80:81], v[100:101], v[72:73] op_sel_hi:[1,0,1]\n"
        "v_pk_fma_f32 v[74:75], v[82:83], v[100:101], v[74:75] op_sel_hi:[1,0,1]\n"
        "v_pk_fma_f32 v[72:73], v[84:85], v[72:73], s[48:49]\n"
        "v_pk_fma_f32 v[74:75], v[86:87], v[74:75], s[50:51]\n"
        "ds_add_u64 v108, v[72:73] offset:22072\n"
        "ds_add_u64 v108, v[74:75] offset:32072\n"
        "s_mov_b64 exec, s[88:89]\n"
        "s_waitcnt lgkmcnt(4)\n"
        "v_cmp_le_u32_e32 vcc, s56, v112\n"
        "s_cbranch_vccz .Lg8_%=\n"
        "s_mov_b64 exec, vcc\n"
        "ds_add_rtn_u32 v112, v115, v114\n"
        "s_mov_b64 exec, s[88:89]\n"
        "s_waitcnt lgkmcnt(0)\n"
        ".Lg8_%=:\n"
        "v_and_b32 v112, 0x7ffff, v112\n"
        "ds_write_b64 v112, v[26:27]\n"
        "s_mov_b64 exec, s[90:91]\n"
        "s_waitcnt lgkmcnt(4)\n"
        "v_cmp_le_u32_e32 vcc, s56, v113\n"
        "s_cbranch_vccz .Lg9_%=\n"
        "s_mov_b64 exec, vcc\n"
        "ds_add_rtn_u32 v113, v115, v114\n"
        "s_mov_b64 exec, s[90:91]\n"
        "s_waitcnt lgkmcnt(0)\n"
        ".Lg9_%=:\n"
        "v_and_b32 v113, 0x7ffff, v113\n"
        "ds_write_b64 v113, v[28:29]\n"
        "s_mov_b64 exec, -1\n"
        "v_add_u32 v102, v6, v102\n"
        "v_add_u32 v103, v6, v103\n"
        "v_add_u32 v104, v6, v104\n"
        "v_add_u32 v105, v6, v105\n"
        "v_add_u32 v106, v6, v106\n"
        "v_add_u32 v107, v6, v107\n"
        "s_setprio 0\n"
        "v_bfe_u32 v101, v57, 14, 17\n"
        "v_pk_mul_f32 v[98:99], v[52:53], s[44:45] op_sel_hi:[0,1]\n"
        "v_mul_f32 v100, s46, v52\n"
        "s_waitcnt lgkmcnt(4)\n"
        "v_mfma_f32_32x32x16_bf16 v[72:87], v[92:95], v[88:91], 0\n"
        "v_cos_f32 v98, v98\n"
        "v_cos_f32 v99, v99\n"
        "v_cos_f32 v100, v100\n"
        "s_mov_b64 exec, s[76:77]\n"
        "v_and_b32 v108, 0x3ff8, v55\n"
        "v_add_u32 v111, v108, v108\n"
        "ds_read_b128 v[88:91], v111 offset:0\n"
        "s_mov_b64 exec, s[78:79]\n"
        "buffer_load_dwordx2 v[66:67], v101, s[40:43], 0 idxen\n"
        "s_mov_b64 exec, -1\n"
        "s_waitcnt vmcnt(2)\n"
        "s_nop 0\n"
        "v_mfma_f32_4x4x4_16b_bf16 v[84:87], v[96:97], v[68:69], 0\n"
        "s_mov_b64 exec, s[74:75]\n"
        "v_pk_mul_f32 v[72:73], v[72:73], v[98:99] op_sel_hi:[1,0]\n"
        "v_pk_mul_f32 v[74:75], v[74:75], v[98:99] op_sel_hi:[1,0]\n"
        "v_pk_fma_f32 v[72:73], v[76:77], v[98:99], v[72:73] op_sel:[0,1,0] op_sel_hi:[1,1,1]\n"
        "v_pk_fma_f32 v[74:75], v[78:79], v[98:99], v[74:75] op_sel:[0,1,0] op_sel_hi:[1,1,1]\n"
        "v_pk_fma_f32 v[72:73], v[80:81], v[100:101], v[72:73] op_sel_hi:[1,0,1]\n"
        "v_pk_fma_f32 v[74:75], v[82:83], v[100:101], v[74:75] op_sel_hi:[1,0,1]\n"
        "v_pk_fma_f32 v[72:73], v[84:85], v[72:73], s[48:49]\n"
        "v_pk_fma_f32 v[74:75], v[86:87], v[74:75], s[50:51]\n"
        "ds_add_u64 v109, v[72:73] offset:22072\n"
        "ds_add_u64 v109, v[74:75] offset:32072\n"
        "s_mov_b64 exec, -1\n"
        "v_cmp_ne_u32_e32 vcc, 0, v33\n"
        "v_and_b32 v110, 0x7ffc0000, v31\n"
        "v_cmp_class_f32_e64 s[88:89], v31, %[cls]\n"
        "v_cndmask_b32_e32 v33, v30, v32, vcc\n"
        "v_mul_hi_u32 v110, v110, s47\n"
        "v_cndmask_b32_sdwa v32, v32, v30, vcc dst_sel:DWORD dst_unused:UNUSED_PAD src0_sel:WORD_0 src1_sel:WORD_1\n"
        "v_and_b32 v111, 0x7ffc0000, v33\n"
        "v_cmp_class_f32_e64 s[90:91], v33, %[cls]\n"
        "v_mul_hi_u32 v111, v111, s47\n"
        "v_and_b32 v110, -4, v110\n"
        "v_and_b32 v111, -4, v111\n"
        "s_mov_b64 exec, s[88:89]\n"
        "ds_add_rtn_u32 v112, v110, v114 offset:20016\n"
        "s_mov_b64 exec, s[90:91]\n"
        "ds_add_rtn_u32 v113, v111, v114 offset:20016\n"
        "s_mov_b64 exec, -1\n"
        "v_perm_b32 v30, v31, v32, s92\n"
        "v_perm_b32 v32, v33, v32, s93\n"
        "v_and_b32 v30, 0x7ff0000, v30\n"
        "v_and_b32 v32, 0x7ff0000, v32\n"
        "v_bfe_u32 v101, v59, 14, 17\n"
        "v_pk_mul_f32 v[98:99], v[54:55], s[44:45] op_sel_hi:[0,1]\n"
        "v_mul_f32 v100, s46, v54\n"
        "s_waitcnt lgkmcnt(4)\n"
        "v_mfma_f32_32x32x16_bf16 v[72:87], v[92:95], v[88:91], 0\n"
        "v_cos_f32 v98, v98\n"
        "v_cos_f32 v99, v99\n"
        "v_cos_f32 v100, v100\n"
        "s_mov_b64 exec, s[78:79]\n"
        "v_and_b32 v109, 0x3ff8, v57\n"
        "v_add_u32 v111, v109, v109\n"
        "ds_read_b128 v[88:91], v111 offset:0\n"
        "s_mov_b64 exec, s[80:81]\n"
        "buffer_load_dwordx2 v[68:69], v101, s[40:43], 0 idxen\n"
        "s_mov_b64 exec, -1\n"
        "s_waitcnt vmcnt(2)\n"
        "s_nop 0\n"
        "v_mfma_f32_4x4x4_16b_bf16 v[84:87], v[96:97], v[70:71], 0\n"
        "s_mov_b64 exec, s[76:77]\n"
        "v_pk_mul_f32 v[72:73], v[72:73], v[98:99] op_sel_hi:[1,0]\n"
        "v_pk_mul_f32 v[74:75], v[74:75], v[98:99] op_sel_hi:[1,0]\n"
        "v_pk_fma_f32 v[72:73], v[76:77], v[98:99], v[72:73] op_sel:[0,1,0] op_sel_hi:[1,1,1]\n"
        "v_pk_fma_f32 v[74:75], v[78:79], v[98:99], v[74:75] op_sel:[0,1,0] op_sel_hi:[1,1,1]\n"
        "v_pk_fma_f32 v[72:73], v[80:81], v[100:101], v[72:73] op_sel_hi:[1,0,1]\n"
        "v_pk_fma_f32 v[74:75], v[82:83], v[100:101], v[74:75] op_sel_hi:[1,0,1]\n"
        "v_pk_fma_f32 v[72:73], v[84:85], v[72:73], s[48:49]\n"
        "v_pk_fma_f32 v[74:75], v[86:87], v[74:75], s[50:51]\n"
        "ds_add_u64 v108, v[72:73] offset:22072\n"
        "ds_add_u64 v108, v[74:75] offset:32072\n"
        "s_mov_b64 exec, s[88:89]\n"
        "s_waitcnt lgkmcnt(4)\n"
        "v_cmp_le_u32_e32 vcc, s56, v112\n"
        "s_cbranch_vccz .Lg10_%=\n"
        "s_mov_b64 exec, vcc\n"
        "ds_add_rtn_u32 v112, v115, v114\n"
        "s_mov_b64 exec, s[88:89]\n"
        "s_waitcnt lgkmcnt(0)\n"
        ".Lg10_%=:\n"
        "v_and_b32 v112, 0x7ffff, v112\n"
        "ds_write_b64 v112, v[30:31]\n"
        "s_mov_b64 exec, s[90:91]\n"
        "s_waitcnt lgkmcnt(4)\n"
        "v_cmp_le_u32_e32 vcc, s56, v113\n"
        "s_cbranch_vccz .Lg11_%=\n"
        "s_mov_b64 exec, vcc\n"
        "ds_add_rtn_u32 v113, v115, v114\n"
        "s_mov_b64 exec, s[90:91]\n"
        "s_waitcnt lgkmcnt(0)\n"
        ".Lg11_%=:\n"
        "v_and_b32 v113, 0x7ffff, v113\n"
        "ds_write_b64 v113, v[32:33]\n"
        "s_mov_b64 exec, -1\n"
        "v_cmp_ne_u32_e32 vcc, 0, v37\n"
        "v_and_b32 v110, 0x7ffc0000, v35\n"
        "v_cmp_class_f32_e64 s[88:89], v35, %[cls]\n"
        "v_cndmask_b32_e32 v37, v34, v36, vcc\n"
        "v_mul_hi_u32 v110, v110, s47\n"
        "v_cndmask_b32_sdwa v36, v36, v34, vcc dst_sel:DWORD dst_unused:UNUSED_PAD src0_sel:WORD_0 src1_sel:WORD_1\n"
        "v_and_b32 v111, 0x7ffc0000, v37\n"
        "v_cmp_class_f32_e64 s[90:91], v37, %[cls]\n"
        "v_mul_hi_u32 v111, v111, s47\n"
        "v_and_b32 v110, -4, v110\n"
        "v_and_b32 v111, -4, v111\n"
        "s_mov_b64 exec, s[88:89]\n"
        "ds_add_rtn_u32 v112, v110, v114 offset:20016\n"
        "s_mov_b64 exec, s[90:91]\n"
        "ds_add_rtn_u32 v113, v111, v114 offset:20016\n"
        "s_mov_b64 exec, -1\n"
        "v_perm_b32 v34, v35, v36, s92\n"
        "v_perm_b32 v36, v37, v36, s93\n"
        "v_and_b32 v34, 0x7ff0000, v34\n"
        "v_and_b32 v36, 0x7ff0000, v36\n"
        "v_bfe_u32 v101, v61, 14, 17\n"
        "v_pk_mul_f32 v[98:99], v[56:57], s[44:45] op_sel_hi:[0,1]\n"
        "v_mul_f32 v100, s46, v56\n"
        "s_waitcnt lgkmcnt(6)\n"
        "v_mfma_f32_32x32x16_bf16 v[72:87], v[92:95], v[88:91], 0\n"
        "v_cos_f32 v98, v98\n"
        "v_cos_f32 v99, v99\n"
        "v_cos_f32 v100, v100\n"
        "s_mov_b64 exec, s[80:81]\n"
        "v_and_b32 v108, 0x3ff8, v59\n"
        "v_add_u32 v111, v108, v108\n"
        "ds_read_b128 v[88:91], v111 offset:0\n"
        "s_mov_b64 exec, s[82:83]\n"
        "buffer_load_dwordx2 v[70:71], v101, s[40:43], 0 idxen\n"
        "s_mov_b64 exec, -1\n"
        "s_waitcnt vmcnt(2)\n"
        "s_nop 0\n"
        "v_mfma_f32_4x4x4_16b_bf16 v[84:87], v[96:97], v[66:67], 0\n"
        "s_mov_b64 exec, s[78:79]\n"
        "v_pk_mul_f32 v[72:73], v[72:73], v[98:99] op_sel_hi:[1,0]\n"
        "v_pk_mul_f32 v[74:75], v[74:75], v[98:99] op_sel_hi:[1,0]\n"
        "v_pk_fma_f32 v[72:73], v[76:77], v[98:99], v[72:73] op_sel:[0,1,0] op_sel_hi:[1,1,1]\n"
        "v_pk_fma_f32 v[74:75], v[78:79], v[98:99], v[74:75] op_sel:[0,1,0] op_sel_hi:[1,1,1]\n"
        "v_pk_fma_f32 v[72:73], v[80:81], v[100:101], v[72:73] op_sel_hi:[1,0,1]\n"
        "v_pk_fma_f32 v[74:75], v[82:83], v[100:101], v[74:75] op_sel_hi:[1,0,1]\n"
        "v_pk_fma_f32 v[72:73], v[84:85], v[72:73], s[48:49]\n"
        "v_pk_fma_f32 v[74:75], v[86:87], v[74:75], s[50:51]\n"
        "ds_add_u64 v109, v[72:73] offset:22072\n"
        "ds_add_u64 v109, v[74:75] offset:32072\n"
        "s_mov_b64 exec, s[88:89]\n"
        "s_waitcnt lgkmcnt(4)\n"
        "v_cmp_le_u32_e32 vcc, s56, v112\n"
        "s_cbranch_vccz .Lg12_%=\n"
        "s_mov_b64 exec, vcc\n"
        "ds_add_rtn_u32 v112, v115, v114\n"
        "s_mov_b64 exec, s[88:89]\n"
        "s_waitcnt lgkmcnt(0)\n"
        ".Lg12_%=:\n"
        "v_and_b32 v112, 0x7ffff, v112\n"
        "ds_write_b64 v112, v[34:35]\n"
        "s_mov_b64 exec, s[90:91]\n"
        "s_waitcnt lgkmcnt(4)\n"
        "v_cmp_le_u32_e32 vcc, s56, v113\n"
        "s_cbranch_vccz .Lg13_%=\n"
        "s_mov_b64 exec, vcc\n"
        "ds_add_rtn_u32 v113, v115, v114\n"
        "s_mov_b64 exec, s[90:91]\n"
        "s_waitcnt lgkmcnt(0)\n"
        ".Lg13_%=:\n"
        "v_and_b32 v113, 0x7ffff, v113\n"
        "ds_write_b64 v113, v[36:37]\n"
        "s_mov_b64 exec, -1\n"
        "s_bitcmp1_b32 %[big], 1\n"
        "s_cbranch_scc0 .Lg14_%=\n"
        "s_waitcnt vmcnt(0) lgkmcnt(0)\n"
        "ds_read_b32 v111, v11 offset:208\n"
        "s_waitcnt lgkmcnt(0)\n"
        "v_readlane_b32 s54, v111, 12\n"
        "v_readlane_b32 s55, v111, 13\n"
        "s_nop 0\n"
        "v_mbcnt_lo_u32_b32 v110, s54, 0\n"
        "v_mbcnt_hi_u32_b32 v110, s55, v110\n"
        "s_bcnt1_i32_b64 s54, s[54:55]\n"
        "s_sub_u32 s54, 32, s54\n"
        "s_lshl2_add_u32 s52, s54, s53\n"
        "v_lshl_add_u32 v110, v110, 2, s52\n"
        "ds_read_b32 v110, v110 offset:272\n"
        "s_waitcnt lgkmcnt(0)\n"
        "v_add_u32 v110, v6, v110\n"
        "v_add_u32 v110, 3840, v110\n"
        "v_and_b32 v17, 2, v110\n"
        "v_and_b32 v110, -4, v110\n"
        "buffer_load_dwordx3 v[14:16], v110, s[36:39], 0 offen sc1\n"
        "v_cmp_ne_u32_e32 vcc, 0, v17\n"
        "s_waitcnt vmcnt(0)\n"
        "v_and_b32 v110, 0x7ffc0000, v15\n"
        "v_cmp_class_f32_e64 s[88:89], v15, %[cls]\n"
        "v_cndmask_b32_e32 v17, v14, v16, vcc\n"
        "v_mul_hi_u32 v110, v110, s47\n"
        "v_cndmask_b32_sdwa v16, v16, v14, vcc dst_sel:DWORD dst_unused:UNUSED_PAD src0_sel:WORD_0 src1_sel:WORD_1\n"
        "v_and_b32 v111, 0x7ffc0000, v17\n"
        "v_cmp_class_f32_e64 s[90:91], v17, %[cls]\n"
        "v_mul_hi_u32 v111, v111, s47\n"
        "v_and_b32 v110, -4, v110\n"
        "v_and_b32 v111, -4, v111\n"
        "s_mov_b64 exec, s[88:89]\n"
        "ds_add_rtn_u32 v112, v110, v114 offset:20016\n"
        "s_mov_b64 exec, s[90:91]\n"
        "ds_add_rtn_u32 v113, v111, v114 offset:20016\n"
        "s_mov_b64 exec, -1\n"
        "v_perm_b32 v14, v15, v16, s92\n"
        "v_perm_b32 v16, v17, v16, s93\n"
        "v_and_b32 v14, 0x7ff0000, v14\n"
        "v_and_b32 v16, 0x7ff0000, v16\n"
        "s_mov_b64 exec, s[88:89]\n"
        "s_waitcnt lgkmcnt(1)\n"
        "v_cmpx_le_u32_e32 vcc, s56, v112\n"
        "ds_add_rtn_u32 v112, v115, v114\n"
        "s_mov_b64 exec, s[90:91]\n"
        "s_waitcnt lgkmcnt(1)\n"
        "v_cmpx_le_u32_e32 vcc, s56, v113\n"
        "ds_add_rtn_u32 v113, v115, v114\n"
        "s_mov_b64 exec, -1\n"
        "s_nop 1\n"
        "s_mov_b64 exec, s[88:89]\n"
        "s_waitcnt lgkmcnt(1)\n"
        "v_and_b32 v112, 0x7ffff, v112\n"
        "ds_write_b64 v112, v[14:15]\n"
        "s_mov_b64 exec, s[90:91]\n"
        "s_waitcnt lgkmcnt(1)\n"
        "v_and_b32 v113, 0x7ffff, v113\n"
        "ds_write_b64 v113, v[16:17]\n"
        "s_mov_b64 exec, -1\n"
        "s_waitcnt lgkmcnt(0)\n"
        ".Lg14_%=:\n"
        "s_nop 1\n"
        "v_and_b32 v17, 2, v102\n"
        "v_and_b32 v102, -4, v102\n"
        "buffer_load_dwordx3 v[14:16], v102, s[36:39], 0 offen offset:0 sc1\n"
        "v_and_b32 v21, 2, v103\n"
        "v_and_b32 v103, -4, v103\n"
        "buffer_load_dwordx3 v[18:20], v103, s[36:39], 0 offen offset:640 sc1\n"
        "v_and_b32 v25, 2, v104\n"
        "v_and_b32 v104, -4, v104\n"
        "buffer_load_dwordx3 v[22:24], v104, s[36:39], 0 offen offset:1280 sc1\n"
        "v_and_b32 v29, 2, v105\n"
        "v_and_b32 v105, -4, v105\n"
        "buffer_load_dwordx3 v[26:28], v105, s[36:39], 0 offen offset:1920 sc1\n"
        "v_and_b32 v33, 2, v106\n"
        "v_and_b32 v106, -4, v106\n"
        "buffer_load_dwordx3 v[30:32], v106, s[36:39], 0 offen offset:2560 sc1\n"
        "v_and_b32 v37, 2, v107\n"
        "v_and_b32 v107, -4, v107\n"
        "buffer_load_dwordx3 v[34:36], v107, s[36:39], 0 offen offset:3200 sc1\n"
        "s_waitcnt lgkmcnt(0)\n"
        "s_barrier\n"
        "s_setprio 3\n"
        "s_add_u32 s40, s40, 640000\n"
        "s_addc_u32 s41, s41, 0\n"
        "s_sub_u32 s42, s42, 80000\n"
        "ds_read_b32 v110, v8\n"
        "ds_read_b64 v[62:63], v7 offset:0\n"
        "ds_read_b64 v[64:65], v7 offset:520\n"
        "ds_read_b64 v[38:39], v7 offset:1040\n"
        "ds_read_b64 v[40:41], v7 offset:1560\n"
        "ds_read_b64 v[42:43], v7 offset:2080\n"
        "ds_read_b64 v[44:45], v7 offset:2600\n"
        "ds_read_b64 v[46:47], v7 offset:3120\n"
        "ds_read_b64 v[48:49], v7 offset:3640\n"
        "ds_read_b64 v[50:51], v7 offset:4160\n"
        "v_subrev_u32 v111, %[ovd], v7\n"
        "ds_read_b64 v[52:53], v111 offset:4680\n"
        "v_subrev_u32 v111, %[ovd], v111\n"
        "ds_read_b64 v[54:55], v111 offset:5200\n"
        "v_subrev_u32 v111, %[ovd], v111\n"
        "ds_read_b64 v[56:57], v111 offset:5720\n"
        "ds_write_b32 v8, v9\n"
        "s_waitcnt lgkmcnt(13)\n"
        "v_and_b32 v110, 0xffffff, v110\n"
        "v_sub_u32 v110, v110, v10\n"
        "v_ashrrev_i32 v110, 3, v110\n"
        "v_add_u32 v12, v12, v110\n"
        "v_max_i32 v13, v13, v110\n"
        "v_med3_i32 v110, v110, 0, 63\n"
        "s_nop 0\n"
        "v_readlane_b32 s84, v110, 0\n"
        "v_readlane_b32 s86, v110, 1\n"
        "v_readlane_b32 s60, v110, 2\n"
        "v_readlane_b32 s62, v110, 3\n"
        "v_readlane_b32 s64, v110, 4\n"
        "v_readlane_b32 s66, v110, 5\n"
        "v_readlane_b32 s68, v110, 6\n"
        "v_readlane_b32 s70, v110, 7\n"
        "v_readlane_b32 s72, v110, 8\n"
        "v_readlane_b32 s74, v110, 9\n"
        "v_readlane_b32 s76, v110, 10\n"
        "v_readlane_b32 s78, v110, 11\n"
        "s_bfm_b64 s[84:85], s84, 0\n"
        "s_bfm_b64 s[86:87], s86, 0\n"
        "s_bfm_b64 s[60:61], s60, 0\n"
        "s_bfm_b64 s[62:63], s62, 0\n"
        "s_bfm_b64 s[64:65], s64, 0\n"
        "s_bfm_b64 s[66:67], s66, 0\n"
        "s_bfm_b64 s[68:69], s68, 0\n"
        "s_bfm_b64 s[70:71], s70, 0\n"
        "s_bfm_b64 s[72:73], s72, 0\n"
        "s_bfm_b64 s[74:75], s74, 0\n"
        "s_bfm_b64 s[76:77], s76, 0\n"
        "s_bfm_b64 s[78:79], s78, 0\n"
        "s_waitcnt lgkmcnt(12)\n"
        "v_bfe_u32 v101, v63, 14, 17\n"
        "v_pk_mul_f32 v[98:99], v[58:59], s[44:45] op_sel_hi:[0,1]\n"
        "v_mul_f32 v100, s46, v58\n"
        "v_mfma_f32_32x32x16_bf16 v[72:87], v[92:95], v[88:91], 0\n"
        "v_cos_f32 v98, v98\n"
        "v_cos_f32 v99, v99\n"
        "v_cos_f32 v100, v100\n"
        "s_mov_b64 exec, s[82:83]\n"
        "v_and_b32 v109, 0x3ff8, v61\n"
        "v_add_u32 v111, v109, v109\n"
        "ds_read_b128 v[88:91], v111 offset:0\n"
        "s_mov_b64 exec, s[84:85]\n"
        "buffer_load_dwordx2 v[66:67], v101, s[40:43], 0 idxen\n"
        "s_mov_b64 exec, -1\n"
        "s_waitcnt vmcnt(8)\n"
        "s_nop 0\n"
        "v_mfma_f32_4x4x4_16b_bf16 v[84:87], v[96:97], v[68:69], 0\n"
        "s_mov_b64 exec, s[80:81]\n"
        "v_pk_mul_f32 v[72:73], v[72:73], v[98:99] op_sel_hi:[1,0]\n"
        "v_pk_mul_f32 v[74:75], v[74:75], v[98:99] op_sel_hi:[1,0]\n"
        "v_pk_fma_f32 v[72:73], v[76:77], v[98:99], v[72:73] op_sel:[0,1,0] op_sel_hi:[1,1,1]\n"
        "v_pk_fma_f32 v[74:75], v[78:79], v[98:99], v[74:75] op_sel:[0,1,0] op_sel_hi:[1,1,1]\n"
        "v_pk_fma_f32 v[72:73], v[80:81], v[100:101], v[72:73] op_sel_hi:[1,0,1]\n"
        "v_pk_fma_f32 v[74:75], v[82:83], v[100:101], v[74:75] op_sel_hi:[1,0,1]\n"
        "v_pk_fma_f32 v[72:73], v[84:85], v[72:73], s[48:49]\n"
        "v_pk_fma_f32 v[74:75], v[86:87], v[74:75], s[50:51]\n"
        "ds_add_u64 v108, v[72:73] offset:22072\n"
        "ds_add_u64 v108, v[74:75] offset:32072\n"
        "s_mov_b64 exec, -1\n"
        "s_waitcnt lgkmcnt(3)\n"
        "s_barrier\n"
        "v_bfe_u32 v101, v65, 14, 17\n"
        "v_pk_mul_f32 v[98:99], v[60:61], s[44:45] op_sel_hi:[0,1]\n"
        "v_mul_f32 v100, s46, v60\n"
        "s_waitcnt lgkmcnt(2)\n"
        "v_mfma_f32_32x32x16_bf16 v[72:87], v[92:95], v[88:91], 0\n"
        "v_cos_f32 v98, v98\n"
        "v_cos_f32 v99, v99\n"
        "v_cos_f32 v100, v100\n"
        "s_mov_b64 exec, s[84:85]\n"
        "v_and_b32 v108, 0x3ff8, v63\n"
        "v_add_u32 v111, v108, v108\n"
        "ds_read_b128 v[88:91], v111 offset:0\n"
        "s_mov_b64 exec, s[86:87]\n"
        "buffer_load_dwordx2 v[68:69], v101, s[40:43], 0 idxen\n"
        "s_mov_b64 exec, -1\n"
        "s_waitcnt vmcnt(8)\n"
        "s_nop 0\n"
        "v_mfma_f32_4x4x4_16b_bf16 v[84:87], v[96:97], v[70:71], 0\n"
        "s_mov_b64 exec, s[82:83]\n"
        "v_pk_mul_f32 v[72:73], v[72:73], v[98:99] op_sel_hi:[1,0]\n"
        "v_pk_mul_f32 v[74:75], v[74:75], v[98:99] op_sel_hi:[1,0]\n"
        "v_pk_fma_f32 v[72:73], v[76:77], v[98:99], v[72:73] op_sel:[0,1,0] op_sel_hi:[1,1,1]\n"
        "v_pk_fma_f32 v[74:75], v[78:79], v[98:99], v[74:75] op_sel:[0,1,0] op_sel_hi:[1,1,1]\n"
        "v_pk_fma_f32 v[72:73], v[80:81], v[100:101], v[72:73] op_sel_hi:[1,0,1]\n"
        "v_pk_fma_f32 v[74:75], v[82:83], v[100:101], v[74:75] op_sel_hi:[1,0,1]\n"
        "v_pk_fma_f32 v[72:73], v[84:85], v[72:73], s[48:49]\n"
        "v_pk_fma_f32 v[74:75], v[86:87], v[74:75], s[50:51]\n"
        "ds_add_u64 v109, v[72:73] offset:22072\n"
        "ds_add_u64 v109, v[74:75] offset:32072\n"
        "s_mov_b64 exec, -1\n"
        "v_cmp_ne_u32_e32 vcc, 0, v17\n"
        "s_waitcnt vmcnt(7)\n"
        "v_and_b32 v110, 0x7ffc0000, v15\n"
        "v_cmp_class_f32_e64 s[88:89], v15, %[cls]\n"
        "v_cndmask_b32_e32 v17, v14, v16, vcc\n"
        "v_mul_hi_u32 v110, v110, s47\n"
        "v_cndmask_b32_sdwa v16, v16, v14, vcc dst_sel:DWORD dst_unused:UNUSED_PAD src0_sel:WORD_0 src1_sel:WORD_1\n"
        "v_and_b32 v111, 0x7ffc0000, v17\n"
        "v_cmp_class_f32_e64 s[90:91], v17, %[cls]\n"
        "v_mul_hi_u32 v111, v111, s47\n"
        "v_and_b32 v110, -4, v110\n"
        "v_and_b32 v111, -4, v111\n"
        "s_mov_b64 exec, s[88:89]\n"
        "ds_add_rtn_u32 v112, v110, v114 offset:20016\n"
        "s_mov_b64 exec, s[90:91]\n"
        "ds_add_rtn_u32 v113, v111, v114 offset:20016\n"
        "s_mov_b64 exec, -1\n"
        "v_perm_b32 v14, v15, v16, s92\n"
        "v_perm_b32 v16, v17, v16, s93\n"
        "v_and_b32 v14, 0x7ff0000, v14\n"
        "v_and_b32 v16, 0x7ff0000, v16\n"
        "v_bfe_u32 v101, v39, 14, 17\n"
        "v_pk_mul_f32 v[98:99], v[62:63], s[44:45] op_sel_hi:[0,1]\n"
        "v_mul_f32 v100, s46, v62\n"
        "s_waitcnt lgkmcnt(4)\n"
        "v_mfma_f32_32x32x16_bf16 v[72:87], v[92:95], v[88:91], 0\n"
        "v_cos_f32 v98, v98\n"
        "v_cos_f32 v99, v99\n"
        "v_cos_f32 v100, v100\n"
        "s_mov_b64 exec, s[86:87]\n"
        "v_and_b32 v109, 0x3ff8, v65\n"
        "v_add_u32 v111, v109, v109\n"
        "ds_read_b128 v[88:91], v111 offset:0\n"
        "s_mov_b64 exec, s[60:61]\n"
        "buffer_load_dwordx2 v[70:71], v101, s[40:43], 0 idxen\n"
        "s_mov_b64 exec, -1\n"
        "s_waitcnt vmcnt(2)\n"
        "s_nop 0\n"
        "v_mfma_f32_4x4x4_16b_bf16 v[84:87], v[96:97], v[66:67], 0\n"
        "s_mov_b64 exec, s[84:85]\n"
        "v_pk_mul_f32 v[72:73], v[72:73], v[98:99] op_sel_hi:[1,0]\n"
        "v_pk_mul_f32 v[74:75], v[74:75], v[98:99] op_sel_hi:[1,0]\n"
        "v_pk_fma_f32 v[72:73], v[76:77], v[98:99], v[72:73] op_sel:[0,1,0] op_sel_hi:[1,1,1]\n"
        "v_pk_fma_f32 v[74:75], v[78:79], v[98:99], v[74:75] op_sel:[0,1,0] op_sel_hi:[1,1,1]\n"
        "v_pk_fma_f32 v[72:73], v[80:81], v[100:101], v[72:73] op_sel_hi:[1,0,1]\n"
        "v_pk_fma_f32 v[74:75], v[82:83], v[100:101], v[74:75] op_sel_hi:[1,0,1]\n"
        "v_pk_fma_f32 v[72:73], v[84:85], v[72:73], s[48:49]\n"
        "v_pk_fma_f32 v[74:75], v[86:87], v[74:75], s[50:51]\n"
        "ds_add_u64 v108, v[72:73] offset:22072\n"
        "ds_add_u64 v108, v[74:75] offset:32072\n"
        "s_mov_b64 exec, s[88:89]\n"
        "s_waitcnt lgkmcnt(4)\n"
        "v_cmp_le_u32_e32 vcc, s56, v112\n"
        "s_cbranch_vccz .Lg15_%=\n"
        "s_mov_b64 exec, vcc\n"
        "ds_add_rtn_u32 v112, v115, v114\n"
        "s_mov_b64 exec, s[88:89]\n"
        "s_waitcnt lgkmcnt(0)\n"
        ".Lg15_%=:\n"
        "v_and_b32 v112, 0x7ffff, v112\n"
        "ds_write_b64 v112, v[14:15]\n"
        "s_mov_b64 exec, s[90:91]\n"
        "s_waitcnt lgkmcnt(4)\n"
        "v_cmp_le_u32_e32 vcc, s56, v113\n"
        "s_cbranch_vccz .Lg16_%=\n"
        "s_mov_b64 exec, vcc\n"
        "ds_add_rtn_u32 v113, v115, v114\n"
        "s_mov_b64 exec, s[90:91]\n"
        "s_waitcnt lgkmcnt(0)\n"
        ".Lg16_%=:\n"
        "v_and_b32 v113, 0x7ffff, v113\n"
        "ds_write_b64 v113, v[16:17]\n"
        "s_mov_b64 exec, -1\n"
        "s_setprio 2\n"
        "v_bfe_u32 v101, v41, 14, 17\n"
        "v_pk_mul_f32 v[98:99], v[64:65], s[44:45] op_sel_hi:[0,1]\n"
        "v_mul_f32 v100, s46, v64\n"
        "s_waitcnt lgkmcnt(4)\n"
        "v_mfma_f32_32x32x16_bf16 v[72:87], v[92:95], v[88:91], 0\n"
        "v_cos_f32 v98, v98\n"
        "v_cos_f32 v99, v99\n"
        "v_cos_f32 v100, v100\n"
        "s_mov_b64 exec, s[60:61]\n"
        "v_and_b32 v108, 0x3ff8, v39\n"
        "v_add_u32 v111, v108, v108\n"
        "ds_read_b128 v[88:91], v111 offset:0\n"
        "s_mov_b64 exec, s[62:63]\n"
        "buffer_load_dwordx2 v[66:67], v101, s[40:43], 0 idxen\n"
        "s_mov_b64 exec, -1\n"
        "s_waitcnt vmcnt(2)\n"
        "s_nop 0\n"
        "v_mfma_f32_4x4x4_16b_bf16 v[84:87], v[96:97], v[68:69], 0\n"
        "s_mov_b64 exec, s[86:87]\n"
        "v_pk_mul_f32 v[72:73], v[72:73], v[98:99] op_sel_hi:[1,0]\n"
        "v_pk_mul_f32 v[74:75], v[74:75], v[98:99] op_sel_hi:[1,0]\n"
        "v_pk_fma_f32 v[72:73], v[76:77], v[98:99], v[72:73] op_sel:[0,1,0] op_sel_hi:[1,1,1]\n"
        "v_pk_fma_f32 v[74:75], v[78:79], v[98:99], v[74:75] op_sel:[0,1,0] op_sel_hi:[1,1,1]\n"
        "v_pk_fma_f32 v[72:73], v[80:81], v[100:101], v[72:73] op_sel_hi:[1,0,1]\n"
        "v_pk_fma_f32 v[74:75], v[82:83], v[100:101], v[74:75] op_sel_hi:[1,0,1]\n"
        "v_pk_fma_f32 v[72:73], v[84:85], v[72:73], s[48:49]\n"
        "v_pk_fma_f32 v[74:75], v[86:87], v[74:75], s[50:51]\n"
        "ds_add_u64 v109, v[72:73] offset:22072\n"
        "ds_add_u64 v109, v[74:75] offset:32072\n"
        "s_mov_b64 exec, -1\n"
        "v_cmp_ne_u32_e32 vcc, 0, v21\n"
        "v_and_b32 v110, 0x7ffc0000, v19\n"
        "v_cmp_class_f32_e64 s[88:89], v19, %[cls]\n"
        "v_cndmask_b32_e32 v21, v18, v20, vcc\n"
        "v_mul_hi_u32 v110, v110, s47\n"
        "v_cndmask_b32_sdwa v20, v20, v18, vcc dst_sel:DWORD dst_unused:UNUSED_PAD src0_sel:WORD_0 src1_sel:WORD_1\n"
        "v_and_b32 v111, 0x7ffc0000, v21\n"
        "v_cmp_class_f32_e64 s[90:91], v21, %[cls]\n"
        "v_mul_hi_u32 v111, v111, s47\n"
        "v_and_b32 v110, -4, v110\n"
        "v_and_b32 v111, -4, v111\n"
        "s_mov_b64 exec, s[88:89]\n"
        "ds_add_rtn_u32 v112, v110, v114 offset:20016\n"
        "s_mov_b64 exec, s[90:91]\n"
        "ds_add_rtn_u32 v113, v111, v114 offset:20016\n"
        "s_mov_b64 exec, -1\n"
        "v_perm_b32 v18, v19, v20, s92\n"
        "v_perm_b32 v20, v21, v20, s93\n"
        "v_and_b32 v18, 0x7ff0000, v18\n"
        "v_and_b32 v20, 0x7ff0000, v20\n"
        "v_bfe_u32 v101, v43, 14, 17\n"
        "v_pk_mul_f32 v[98:99], v[38:39], s[44:45] op_sel_hi:[0,1]\n"
        "v_mul_f32 v100, s46, v38\n"
        "s_waitcnt lgkmcnt(4)\n"
        "v_mfma_f32_32x32x16_bf16 v[72:87], v[92:95], v[88:91], 0\n"
        "v_cos_f32 v98, v98\n"
        "v_cos_f32 v99, v99\n"
        "v_cos_f32 v100, v100\n"
        "s_mov_b64 exec, s[62:63]\n"
        "v_and_b32 v109, 0x3ff8, v41\n"
        "v_add_u32 v111, v109, v109\n"
        "ds_read_b128 v[88:91], v111 offset:0\n"
        "s_mov_b64 exec, s[64:65]\n"
        "buffer_load_dwordx2 v[68:69], v101, s[40:43], 0 idxen\n"
        "s_mov_b64 exec, -1\n"
        "s_waitcnt vmcnt(2)\n"
        "s_nop 0\n"
        "v_mfma_f32_4x4x4_16b_bf16 v[84:87], v[96:97], v[70:71], 0\n"
        "s_mov_b64 exec, s[60:61]\n"
        "v_pk_mul_f32 v[72:73], v[72:73], v[98:99] op_sel_hi:[1,0]\n"
        "v_pk_mul_f32 v[74:75], v[74:75], v[98:99] op_sel_hi:[1,0]\n"
        "v_pk_fma_f32 v[72:73], v[76:77], v[98:99], v[72:73] op_sel:[0,1,0] op_sel_hi:[1,1,1]\n"
        "v_pk_fma_f32 v[74:75], v[78:79], v[98:99], v[74:75] op_sel:[0,1,0] op_sel_hi:[1,1,1]\n"
        "v_pk_fma_f32 v[72:73], v[80:81], v[100:101], v[72:73] op_sel_hi:[1,0,1]\n"
        "v_pk_fma_f32 v[74:75], v[82:83], v[100:101], v[74:75] op_sel_hi:[1,0,1]\n"
        "v_pk_fma_f32 v[72:73], v[84:85], v[72:73], s[48:49]\n"
        "v_pk_fma_f32 v[74:75], v[86:87], v[74:75], s[50:51]\n"
        "ds_add_u64 v108, v[72:73] offset:22072\n"
        "ds_add_u64 v108, v[74:75] offset:32072\n"
        "s_mov_b64 exec, s[88:89]\n"
        "s_waitcnt lgkmcnt(4)\n"
        "v_cmp_le_u32_e32 vcc, s56, v112\n"
        "s_cbranch_vccz .Lg17_%=\n"
        "s_mov_b64 exec, vcc\n"
        "ds_add_rtn_u32 v112, v115, v114\n"
        "s_mov_b64 exec, s[88:89]\n"
        "s_waitcnt lgkmcnt(0)\n"
        ".Lg17_%=:\n"
        "v_and_b32 v112, 0x7ffff, v112\n"
        "ds_write_b64 v112, v[18:19]\n"
        "s_mov_b64 exec, s[90:91]\n"
        "s_waitcnt lgkmcnt(4)\n"
        "v_cmp_le_u32_e32 vcc, s56, v113\n"
        "s_cbranch_vccz .Lg18_%=\n"
        "s_mov_b64 exec, vcc\n"
        "ds_add_rtn_u32 v113, v115, v114\n"
        "s_mov_b64 exec, s[90:91]\n"
        "s_waitcnt lgkmcnt(0)\n"
        ".Lg18_%=:\n"
        "v_and_b32 v113, 0x7ffff, v113\n"
        "ds_write_b64 v113, v[20:21]\n"
        "s_mov_b64 exec, -1\n"
        "ds_read_b32 v107, v11 offset:624\n"
        "v_bfe_u32 v101, v45, 14, 17\n"
        "v_pk_mul_f32 v[98:99], v[40:41], s[44:45] op_sel_hi:[0,1]\n"
        "v_mul_f32 v100, s46, v40\n"
        "s_waitcnt lgkmcnt(5)\n"
        "v_mfma_f32_32x32x16_bf16 v[72:87], v[92:95], v[88:91], 0\n"
        "v_cos_f32 v98, v98\n"
        "v_cos_f32 v99, v99\n"
        "v_cos_f32 v100, v100\n"
        "s_mov_b64 exec, s[64:65]\n"
        "v_and_b32 v108, 0x3ff8, v43\n"
        "v_add_u32 v111, v108, v108\n"
        "ds_read_b128 v[88:91], v111 offset:0\n"
        "s_mov_b64 exec, s[66:67]\n"
        "buffer_load_dwordx2 v[70:71], v101, s[40:43], 0 idxen\n"
        "s_mov_b64 exec, -1\n"
        "s_waitcnt vmcnt(2)\n"
        "s_nop 0\n"
        "v_mfma_f32_4x4x4_16b_bf16 v[84:87], v[96:97], v[66:67], 0\n"
        "s_mov_b64 exec, s[62:63]\n"
        "v_pk_mul_f32 v[72:73], v[72:73], v[98:99] op_sel_hi:[1,0]\n"
        "v_pk_mul_f32 v[74:75], v[74:75], v[98:99] op_sel_hi:[1,0]\n"
        "v_pk_fma_f32 v[72:73], v[76:77], v[98:99], v[72:73] op_sel:[0,1,0] op_sel_hi:[1,1,1]\n"
        "v_pk_fma_f32 v[74:75], v[78:79], v[98:99], v[74:75] op_sel:[0,1,0] op_sel_hi:[1,1,1]\n"
        "v_pk_fma_f32 v[72:73], v[80:81], v[100:101], v[72:73] op_sel_hi:[1,0,1]\n"
        "v_pk_fma_f32 v[74:75], v[82:83], v[100:101], v[74:75] op_sel_hi:[1,0,1]\n"
        "v_pk_fma_f32 v[72:73], v[84:85], v[72:73], s[48:49]\n"
        "v_pk_fma_f32 v[74:75], v[86:87], v[74:75], s[50:51]\n"
        "ds_add_u64 v109, v[72:73] offset:22072\n"
        "ds_add_u64 v109, v[74:75] offset:32072\n"
        "s_mov_b64 exec, -1\n"
        "v_cmp_ne_u32_e32 vcc, 0, v25\n"
        "v_and_b32 v110, 0x7ffc0000, v23\n"
        "v_cmp_class_f32_e64 s[88:89], v23, %[cls]\n"
        "v_cndmask_b32_e32 v25, v22, v24, vcc\n"
        "v_mul_hi_u32 v110, v110, s47\n"
        "v_cndmask_b32_sdwa v24, v24, v22, vcc dst_sel:DWORD dst_unused:UNUSED_PAD src0_sel:WORD_0 src1_sel:WORD_1\n"
        "v_and_b32 v111, 0x7ffc0000, v25\n"
        "v_cmp_class_f32_e64 s[90:91], v25, %[cls]\n"
        "v_mul_hi_u32 v111, v111, s47\n"
        "v_and_b32 v110, -4, v110\n"
        "v_and_b32 v111, -4, v111\n"
        "s_mov_b64 exec, s[88:89]\n"
        "ds_add_rtn_u32 v112, v110, v114 offset:20016\n"
        "s_mov_b64 exec, s[90:91]\n"
        "ds_add_rtn_u32 v113, v111, v114 offset:20016\n"
        "s_mov_b64 exec, -1\n"
        "v_perm_b32 v22, v23, v24, s92\n"
        "v_perm_b32 v24, v25, v24, s93\n"
        "v_and_b32 v22, 0x7ff0000, v22\n"
        "v_and_b32 v24, 0x7ff0000, v24\n"
        "s_setprio 1\n"
        "v_bfe_u32 v101, v47, 14, 17\n"
        "v_pk_mul_f32 v[98:99], v[42:43], s[44:45] op_sel_hi:[0,1]\n"
        "v_mul_f32 v100, s46, v42\n"
        "s_waitcnt lgkmcnt(4)\n"
        "v_mfma_f32_32x32x16_bf16 v[72:87], v[92:95], v[88:91], 0\n"
        "v_cos_f32 v98, v98\n"
        "v_cos_f32 v99, v99\n"
        "v_cos_f32 v100, v100\n"
        "s_mov_b64 exec, s[66:67]\n"
        "v_and_b32 v109, 0x3ff8, v45\n"
        "v_add_u32 v111, v109, v109\n"
        "ds_read_b128 v[88:91], v111 offset:0\n"
        "s_mov_b64 exec, s[68:69]\n"
        "buffer_load_dwordx2 v[66:67], v101, s[40:43], 0 idxen\n"
        "s_mov_b64 exec, -1\n"
        "s_waitcnt vmcnt(2)\n"
        "s_nop 0\n"
        "v_mfma_f32_4x4x4_16b_bf16 v[84:87], v[96:97], v[68:69], 0\n"
        "s_mov_b64 exec, s[64:65]\n"
        "v_pk_mul_f32 v[72:73], v[72:73], v[98:99] op_sel_hi:[1,0]\n"
        "v_pk_mul_f32 v[74:75], v[74:75], v[98:99] op_sel_hi:[1,0]\n"
        "v_pk_fma_f32 v[72:73], v[76:77], v[98:99], v[72:73] op_sel:[0,1,0] op_sel_hi:[1,1,1]\n"
        "v_pk_fma_f32 v[74:75], v[78:79], v[98:99], v[74:75] op_sel:[0,1,0] op_sel_hi:[1,1,1]\n"
        "v_pk_fma_f32 v[72:73], v[80:81], v[100:101], v[72:73] op_sel_hi:[1,0,1]\n"
        "v_pk_fma_f32 v[74:75], v[82:83], v[100:101], v[74:75] op_sel_hi:[1,0,1]\n"
        "v_pk_fma_f32 v[72:73], v[84:85], v[72:73], s[48:49]\n"
        "v_pk_fma_f32 v[74:75], v[86:87], v[74:75], s[50:51]\n"
        "ds_add_u64 v108, v[72:73] offset:22072\n"
        "ds_add_u64 v108, v[74:75] offset:32072\n"
        "s_mov_b64 exec, s[88:89]\n"
        "s_waitcnt lgkmcnt(4)\n"
        "v_cmp_le_u32_e32 vcc, s56, v112\n"
        "s_cbranch_vccz .Lg19_%=\n"
        "s_mov_b64 exec, vcc\n"
        "ds_add_rtn_u32 v112, v115, v114\n"
        "s_mov_b64 exec, s[88:89]\n"
        "s_waitcnt lgkmcnt(0)\n"
        ".Lg19_%=:\n"
        "v_and_b32 v112, 0x7ffff, v112\n"
        "ds_write_b64 v112, v[22:23]\n"
        "s_mov_b64 exec, s[90:91]\n"
        "s_waitcnt lgkmcnt(4)\n"
        "v_cmp_le_u32_e32 vcc, s56, v113\n"
        "s_cbranch_vccz .Lg20_%=\n"
        "s_mov_b64 exec, vcc\n"
        "ds_add_rtn_u32 v113, v115, v114\n"
        "s_mov_b64 exec, s[90:91]\n"
        "s_waitcnt lgkmcnt(0)\n"
        ".Lg20_%=:\n"
        "v_and_b32 v113, 0x7ffff, v113\n"
        "ds_write_b64 v113, v[24:25]\n"
        "s_mov_b64 exec, -1\n"
        "s_add_u32 s52, s53, 688\n"
        "v_readlane_b32 s54, v107, 0\n"
        "v_readlane_b32 s55, v107, 1\n"
        "s_nop 0\n"
        "v_mbcnt_lo_u32_b32 v110, s54, 0\n"
        "v_mbcnt_hi_u32_b32 v110, s55, v110\n"
        "v_lshl_add_u32 v110, v110, 2, s52\n"
        "ds_read_b32 v102, v110\n"
        "s_bcnt1_i32_b64 s54, s[54:55]\n"
        "s_lshl2_add_u32 s52, s54, s52\n"
        "v_readlane_b32 s54, v107, 2\n"
        "v_readlane_b32 s55, v107, 3\n"
        "s_nop 0\n"
        "v_mbcnt_lo_u32_b32 v110, s54, 0\n"
        "v_mbcnt_hi_u32_b32 v110, s55, v110\n"
        "v_lshl_add_u32 v110, v110, 2, s52\n"
        "ds_read_b32 v103, v110\n"
        "s_bcnt1_i32_b64 s54, s[54:55]\n"
        "s_lshl2_add_u32 s52, s54, s52\n"
        "v_readlane_b32 s54, v107, 4\n"
        "v_readlane_b32 s55, v107, 5\n"
        "s_nop 0\n"
        "v_mbcnt_lo_u32_b32 v110, s54, 0\n"
        "v_mbcnt_hi_u32_b32 v110, s55, v110\n"
        "v_lshl_add_u32 v110, v110, 2, s52\n"
        "ds_read_b32 v104, v110\n"
        "s_bcnt1_i32_b64 s54, s[54:55]\n"
        "s_lshl2_add_u32 s52, s54, s52\n"
        "v_readlane_b32 s54, v107, 6\n"
        "v_readlane_b32 s55, v107, 7\n"
        "s_nop 0\n"
        "v_mbcnt_lo_u32_b32 v110, s54, 0\n"
        "v_mbcnt_hi_u32_b32 v110, s55, v110\n"
        "v_lshl_add_u32 v110, v110, 2, s52\n"
        "ds_read_b32 v105, v110\n"
        "s_bcnt1_i32_b64 s54, s[54:55]\n"
        "s_lshl2_add_u32 s52, s54, s52\n"
        "v_readlane_b32 s54, v107, 8\n"
        "v_readlane_b32 s55, v107, 9\n"
        "s_nop 0\n"
        "v_mbcnt_lo_u32_b32 v110, s54, 0\n"
        "v_mbcnt_hi_u32_b32 v110, s55, v110\n"
        "v_lshl_add_u32 v110, v110, 2, s52\n"
        "ds_read_b32 v106, v110\n"
        "s_bcnt1_i32_b64 s54, s[54:55]\n"
        "s_lshl2_add_u32 s52, s54, s52\n"
        "v_readlane_b32 s54, v107, 10\n"
        "v_readlane_b32 s55, v107, 11\n"
        "s_nop 0\n"
        "v_mbcnt_lo_u32_b32 v110, s54, 0\n"
        "v_mbcnt_hi_u32_b32 v110, s55, v110\n"
        "v_lshl_add_u32 v110, v110, 2, s52\n"
        "ds_read_b32 v107, v110\n"
        "v_bfe_u32 v101, v49, 14, 17\n"
        "v_pk_mul_f32 v[98:99], v[44:45], s[44:45] op_sel_hi:[0,1]\n"
        "v_mul_f32 v100, s46, v44\n"
        "s_waitcnt lgkmcnt(10)\n"
        "v_mfma_f32_32x32x16_bf16 v[72:87], v[92:95], v[88:91], 0\n"
        "v_cos_f32 v98, v98\n"
        "v_cos_f32 v99, v99\n"
        "v_cos_f32 v100, v100\n"
        "s_mov_b64 exec, s[68:69]\n"
        "v_and_b32 v108, 0x3ff8, v47\n"
        "v_add_u32 v111, v108, v108\n"
        "ds_read_b128 v[88:91], v111 offset:0\n"
        "s_mov_b64 exec, s[70:71]\n"
        "buffer_load_dwordx2 v[68:69], v101, s[40:43], 0 idxen\n"
        "s_mov_b64 exec, -1\n"
        "s_waitcnt vmcnt(2)\n"
        "s_nop 0\n"
        "v_mfma_f32_4x4x4_16b_bf16 v[84:87], v[96:97], v[70:71], 0\n"
        "s_mov_b64 exec, s[66:67]\n"
        "v_pk_mul_f32 v[72:73], v[72:73], v[98:99] op_sel_hi:[1,0]\n"
        "v_pk_mul_f32 v[74:75], v[74:75], v[98:99] op_sel_hi:[1,0]\n"
        "v_pk_fma_f32 v[72:73], v[76:77], v[98:99], v[72:73] op_sel:[0,1,0] op_sel_hi:[1,1,1]\n"
        "v_pk_fma_f32 v[74:75], v[78:79], v[98:99], v[74:75] op_sel:[0,1,0] op_sel_hi:[1,1,1]\n"
        "v_pk_fma_f32 v[72:73], v[80:81], v[100:101], v[72:73] op_sel_hi:[1,0,1]\n"
        "v_pk_fma_f32 v[74:75], v[82:83], v[100:101], v[74:75] op_sel_hi:[1,0,1]\n"
        "v_pk_fma_f32 v[72:73], v[84:85], v[72:73], s[48:49]\n"
        "v_pk_fma_f32 v[74:75], v[86:87], v[74:75], s[50:51]\n"
        "ds_add_u64 v109, v[72:73] offset:22072\n"
        "ds_add_u64 v109, v[74:75] offset:32072\n"
        "s_mov_b64 exec, -1\n"
        "v_cmp_ne_u32_e32 vcc, 0, v29\n"
        "v_and_b32 v110, 0x7ffc0000, v27\n"
        "v_cmp_class_f32_e64 s[88:89], v27, %[cls]\n"
        "v_cndmask_b32_e32 v29, v26, v28, vcc\n"
        "v_mul_hi_u32 v110, v110, s47\n"
        "v_cndmask_b32_sdwa v28, v28, v26, vcc dst_sel:DWORD dst_unused:UNUSED_PAD src0_sel:WORD_0 src1_sel:WORD_1\n"
        "v_and_b32 v111, 0x7ffc0000, v29\n"
        "v_cmp_class_f32_e64 s[90:91], v29, %[cls]\n"
        "v_mul_hi_u32 v111, v111, s47\n"
        "v_and_b32 v110, -4, v110\n"
        "v_and_b32 v111, -4, v111\n"
        "s_mov_b64 exec, s[88:89]\n"
        "ds_add_rtn_u32 v112, v110, v114 offset:20016\n"
        "s_mov_b64 exec, s[90:91]\n"
        "ds_add_rtn_u32 v113, v111, v114 offset:20016\n"
        "s_mov_b64 exec, -1\n"
        "v_perm_b32 v26, v27, v28, s92\n"
        "v_perm_b32 v28, v29, v28, s93\n"
        "v_and_b32 v26, 0x7ff0000, v26\n"
        "v_and_b32 v28, 0x7ff0000, v28\n"
        "v_bfe_u32 v101, v51, 14, 17\n"
        "v_pk_mul_f32 v[98:99], v[46:47], s[44:45] op_sel_hi:[0,1]\n"
        "v_mul_f32 v100, s46, v46\n"
        "s_waitcnt lgkmcnt(4)\n"
        "v_mfma_f32_32x32x16_bf16 v[72:87], v[92:95], v[88:91], 0\n"
        "v_cos_f32 v98, v98\n"
        "v_cos_f32 v99, v99\n"
        "v_cos_f32 v100, v100\n"
        "s_mov_b64 exec, s[70:71]\n"
        "v_and_b32 v109, 0x3ff8, v49\n"
        "v_add_u32 v111, v109, v109\n"
        "ds_read_b128 v[88:91], v111 offset:0\n"
        "s_mov_b64 exec, s[72:73]\n"
        "buffer_load_dwordx2 v[70:71], v101, s[40:43], 0 idxen\n"
        "s_mov_b64 exec, -1\n"
        "s_waitcnt vmcnt(2)\n"
        "s_nop 0\n"
        "v_mfma_f32_4x4x4_16b_bf16 v[84:87], v[96:97], v[66:67], 0\n"
        "s_mov_b64 exec, s[68:69]\n"
        "v_pk_mul_f32 v[72:73], v[72:73], v[98:99] op_sel_hi:[1,0]\n"
        "v_pk_mul_f32 v[74:75], v[74:75], v[98:99] op_sel_hi:[1,0]\n"
        "v_pk_fma_f32 v[72:73], v[76:77], v[98:99], v[72:73] op_sel:[0,1,0] op_sel_hi:[1,1,1]\n"
        "v_pk_fma_f32 v[74:75], v[78:79], v[98:99], v[74:75] op_sel:[0,1,0] op_sel_hi:[1,1,1]\n"
        "v_pk_fma_f32 v[72:73], v[80:81], v[100:101], v[72:73] op_sel_hi:[1,0,1]\n"
        "v_pk_fma_f32 v[74:75], v[82:83], v[100:101], v[74:75] op_sel_hi:[1,0,1]\n"
        "v_pk_fma_f32 v[72:73], v[84:85], v[72:73], s[48:49]\n"
        "v_pk_fma_f32 v[74:75], v[86:87], v[74:75], s[50:51]\n"
        "ds_add_u64 v108, v[72:73] offset:22072\n"
        "ds_add_u64 v108, v[74:75] offset:32072\n"
        "s_mov_b64 exec, s[88:89]\n"
        "s_waitcnt lgkmcnt(4)\n"
        "v_cmp_le_u32_e32 vcc, s56, v112\n"
        "s_cbranch_vccz .Lg21_%=\n"
        "s_mov_b64 exec, vcc\n"
        "ds_add_rtn_u32 v112, v115, v114\n"
        "s_mov_b64 exec, s[88:89]\n"
        "s_waitcnt lgkmcnt(0)\n"
        ".Lg21_%=:\n"
        "v_and_b32 v112, 0x7ffff, v112\n"
        "ds_write_b64 v112, v[26:27]\n"
        "s_mov_b64 exec, s[90:91]\n"
        "s_waitcnt lgkmcnt(4)\n"
        "v_cmp_le_u32_e32 vcc, s56, v113\n"
        "s_cbranch_vccz .Lg22_%=\n"
        "s_mov_b64 exec, vcc\n"
        "ds_add_rtn_u32 v113, v115, v114\n"
        "s_mov_b64 exec, s[90:91]\n"
        "s_waitcnt lgkmcnt(0)\n"
        ".Lg22_%=:\n"
        "v_and_b32 v113, 0x7ffff, v113\n"
        "ds_write_b64 v113, v[28:29]\n"
        "s_mov_b64 exec, -1\n"
        "v_add_u32 v102, v6, v102\n"
        "v_add_u32 v103, v6, v103\n"
        "v_add_u32 v104, v6, v104\n"
        "v_add_u32 v105, v6, v105\n"
        "v_add_u32 v106, v6, v106\n"
        "v_add_u32 v107, v6, v107\n"
        "s_setprio 0\n"
        "v_bfe_u32 v101, v53, 14, 17\n"
        "v_pk_mul_f32 v[98:99], v[48:49], s[44:45] op_sel_hi:[0,1]\n"
        "v_mul_f32 v100, s46, v48\n"
        "s_waitcnt lgkmcnt(4)\n"
        "v_mfma_f32_32x32x16_bf16 v[72:87], v[92:95], v[88:91], 0\n"
        "v_cos_f32 v98, v98\n"
        "v_cos_f32 v99, v99\n"
        "v_cos_f32 v100, v100\n"
        "s_mov_b64 exec, s[72:73]\n"
        "v_and_b32 v108, 0x3ff8, v51\n"
        "v_add_u32 v111, v108, v108\n"
        "ds_read_b128 v[88:91], v111 offset:0\n"
        "s_mov_b64 exec, s[74:75]\n"
        "buffer_load_dwordx2 v[66:67], v101, s[40:43], 0 idxen\n"
        "s_mov_b64 exec, -1\n"
        "s_waitcnt vmcnt(2)\n"
        "s_nop 0\n"
        "v_mfma_f32_4x4x4_16b_bf16 v[84:87], v[96:97], v[68:69], 0\n"
        "s_mov_b64 exec, s[70:71]\n"
        "v_pk_mul_f32 v[72:73], v[72:73], v[98:99] op_sel_hi:[1,0]\n"
        "v_pk_mul_f32 v[74:75], v[74:75], v[98:99] op_sel_hi:[1,0]\n"
        "v_pk_fma_f32 v[72:73], v[76:77], v[98:99], v[72:73] op_sel:[0,1,0] op_sel_hi:[1,1,1]\n"
        "v_pk_fma_f32 v[74:75], v[78:79], v[98:99], v[74:75] op_sel:[0,1,0] op_sel_hi:[1,1,1]\n"
        "v_pk_fma_f32 v[72:73], v[80:81], v[100:101], v[72:73] op_sel_hi:[1,0,1]\n"
        "v_pk_fma_f32 v[74:75], v[82:83], v[100:101], v[74:75] op_sel_hi:[1,0,1]\n"
        "v_pk_fma_f32 v[72:73], v[84:85], v[72:73], s[48:49]\n"
        "v_pk_fma_f32 v[74:75], v[86:87], v[74:75], s[50:51]\n"
        "ds_add_u64 v109, v[72:73] offset:22072\n"
        "ds_add_u64 v109, v[74:75] offset:32072\n"
        "s_mov_b64 exec, -1\n"
        "v_cmp_ne_u32_e32 vcc, 0, v33\n"
        "v_and_b32 v110, 0x7ffc0000, v31\n"
        "v_cmp_class_f32_e64 s[88:89], v31, %[cls]\n"
        "v_cndmask_b32_e32 v33, v30, v32, vcc\n"
        "v_mul_hi_u32 v110, v110, s47\n"
        "v_cndmask_b32_sdwa v32, v32, v30, vcc dst_sel:DWORD dst_unused:UNUSED_PAD src0_sel:WORD_0 src1_sel:WORD_1\n"
        "v_and_b32 v111, 0x7ffc0000, v33\n"
        "v_cmp_class_f32_e64 s[90:91], v33, %[cls]\n"
        "v_mul_hi_u32 v111, v111, s47\n"
        "v_and_b32 v110, -4, v110\n"
        "v_and_b32 v111, -4, v111\n"
        "s_mov_b64 exec, s[88:89]\n"
        "ds_add_rtn_u32 v112, v110, v114 offset:20016\n"
        "s_mov_b64 exec, s[90:91]\n"
        "ds_add_rtn_u32 v113, v111, v114 offset:20016\n"
        "s_mov_b64 exec, -1\n"
        "v_perm_b32 v30, v31, v32, s92\n"
        "v_perm_b32 v32, v33, v32, s93\n"
        "v_and_b32 v30, 0x7ff0000, v30\n"
        "v_and_b32 v32, 0x7ff0000, v32\n"
        "v_bfe_u32 v101, v55, 14, 17\n"
        "v_pk_mul_f32 v[98:99], v[50:51], s[44:45] op_sel_hi:[0,1]\n"
        "v_mul_f32 v100, s46, v50\n"
        "s_waitcnt lgkmcnt(4)\n"
        "v_mfma_f32_32x32x16_bf16 v[72:87], v[92:95], v[88:91], 0\n"
        "v_cos_f32 v98, v98\n"
        "v_cos_f32 v99, v99\n"
        "v_cos_f32 v100, v100\n"
        "s_mov_b64 exec, s[74:75]\n"
        "v_and_b32 v109, 0x3ff8, v53\n"
        "v_add_u32 v111, v109, v109\n"
        "ds_read_b128 v[88:91], v111 offset:0\n"
        "s_mov_b64 exec, s[76:77]\n"
        "buffer_load_dwordx2 v[68:69], v101, s[40:43], 0 idxen\n"
        "s_mov_b64 exec, -1\n"
        "s_waitcnt vmcnt(2)\n"
        "s_nop 0\n"
        "v_mfma_f32_4x4x4_16b_bf16 v[84:87], v[96:97], v[70:71], 0\n"
        "s_mov_b64 exec, s[72:73]\n"
        "v_pk_mul_f32 v[72:73], v[72:73], v[98:99] op_sel_hi:[1,0]\n"
        "v_pk_mul_f32 v[74:75], v[74:75], v[98:99] op_sel_hi:[1,0]\n"
        "v_pk_fma_f32 v[72:73], v[76:77], v[98:99], v[72:73] op_sel:[0,1,0] op_sel_hi:[1,1,1]\n"
        "v_pk_fma_f32 v[74:75], v[78:79], v[98:99], v[74:75] op_sel:[0,1,0] op_sel_hi:[1,1,1]\n"
        "v_pk_fma_f32 v[72:73], v[80:81], v[100:101], v[72:73] op_sel_hi:[1,0,1]\n"
        "v_pk_fma_f32 v[74:75], v[82:83], v[100:101], v[74:75] op_sel_hi:[1,0,1]\n"
        "v_pk_fma_f32 v[72:73], v[84:85], v[72:73], s[48:49]\n"
        "v_pk_fma_f32 v[74:75], v[86:87], v[74:75], s[50:51]\n"
        "ds_add_u64 v108, v[72:73] offset:22072\n"
        "ds_add_u64 v108, v[74:75] offset:32072\n"
        "s_mov_b64 exec, s[88:89]\n"
        "s_waitcnt lgkmcnt(4)\n"
        "v_cmp_le_u32_e32 vcc, s56, v112\n"
        "s_cbranch_vccz .Lg23_%=\n"
        "s_mov_b64 exec, vcc\n"
        "ds_add_rtn_u32 v112, v115, v114\n"
        "s_mov_b64 exec, s[88:89]\n"
        "s_waitcnt lgkmcnt(0)\n"
        ".Lg23_%=:\n"
        "v_and_b32 v112, 0x7ffff, v112\n"
        "ds_write_b64 v112, v[30:31]\n"
        "s_mov_b64 exec, s[90:91]\n"
        "s_waitcnt lgkmcnt(4)\n"
        "v_cmp_le_u32_e32 vcc, s56, v113\n"
        "s_cbranch_vccz .Lg24_%=\n"
        "s_mov_b64 exec, vcc\n"
        "ds_add_rtn_u32 v113, v115, v114\n"
        "s_mov_b64 exec, s[90:91]\n"
        "s_waitcnt lgkmcnt(0)\n"
        ".Lg24_%=:\n"
        "v_and_b32 v113, 0x7ffff, v113\n"
        "ds_write_b64 v113, v[32:33]\n"
        "s_mov_b64 exec, -1\n"
        "v_cmp_ne_u32_e32 vcc, 0, v37\n"
        "v_and_b32 v110, 0x7ffc0000, v35\n"
        "v_cmp_class_f32_e64 s[88:89], v35, %[cls]\n"
        "v_cndmask_b32_e32 v37, v34, v36, vcc\n"
        "v_mul_hi_u32 v110, v110, s47\n"
        "v_cndmask_b32_sdwa v36, v36, v34, vcc dst_sel:DWORD dst_unused:UNUSED_PAD src0_sel:WORD_0 src1_sel:WORD_1\n"
        "v_and_b32 v111, 0x7ffc0000, v37\n"
        "v_cmp_class_f32_e64 s[90:91], v37, %[cls]\n"
        "v_mul_hi_u32 v111, v111, s47\n"
        "v_and_b32 v110, -4, v110\n"
        "v_and_b32 v111, -4, v111\n"
        "s_mov_b64 exec, s[88:89]\n"
        "ds_add_rtn_u32 v112, v110, v114 offset:20016\n"
        "s_mov_b64 exec, s[90:91]\n"
        "ds_add_rtn_u32 v113, v111, v114 offset:20016\n"
        "s_mov_b64 exec, -1\n"
        "v_perm_b32 v34, v35, v36, s92\n"
        "v_perm_b32 v36, v37, v36, s93\n"
        "v_and_b32 v34, 0x7ff0000, v34\n"
        "v_and_b32 v36, 0x7ff0000, v36\n"
        "v_bfe_u32 v101, v57, 14, 17\n"
        "v_pk_mul_f32 v[98:99], v[52:53], s[44:45] op_sel_hi:[0,1]\n"
        "v_mul_f32 v100, s46, v52\n"
        "s_waitcnt lgkmcnt(6)\n"
        "v_mfma_f32_32x32x16_bf16 v[72:87], v[92:95], v[88:91], 0\n"
        "v_cos_f32 v98, v98\n"
        "v_cos_f32 v99, v99\n"
        "v_cos_f32 v100, v100\n"
        "s_mov_b64 exec, s[76:77]\n"
        "v_and_b32 v108, 0x3ff8, v55\n"
        "v_add_u32 v111, v108, v108\n"
        "ds_read_b128 v[88:91], v111 offset:0\n"
        "s_mov_b64 exec, s[78:79]\n"
        "buffer_load_dwordx2 v[70:71], v101, s[40:43], 0 idxen\n"
        "s_mov_b64 exec, -1\n"
        "s_waitcnt vmcnt(2)\n"
        "s_nop 0\n"
        "v_mfma_f32_4x4x4_16b_bf16 v[84:87], v[96:97], v[66:67], 0\n"
        "s_mov_b64 exec, s[74:75]\n"
        "v_pk_mul_f32 v[72:73], v[72:73], v[98:99] op_sel_hi:[1,0]\n"
        "v_pk_mul_f32 v[74:75], v[74:75], v[98:99] op_sel_hi:[1,0]\n"
        "v_pk_fma_f32 v[72:73], v[76:77], v[98:99], v[72:73] op_sel:[0,1,0] op_sel_hi:[1,1,1]\n"
        "v_pk_fma_f32 v[74:75], v[78:79], v[98:99], v[74:75] op_sel:[0,1,0] op_sel_hi:[1,1,1]\n"
        "v_pk_fma_f32 v[72:73], v[80:81], v[100:101], v[72:73] op_sel_hi:[1,0,1]\n"
        "v_pk_fma_f32 v[74:75], v[82:83], v[100:101], v[74:75] op_sel_hi:[1,0,1]\n"
        "v_pk_fma_f32 v[72:73], v[84:85], v[72:73], s[48:49]\n"
        "v_pk_fma_f32 v[74:75], v[86:87], v[74:75], s[50:51]\n"
        "ds_add_u64 v109, v[72:73] offset:22072\n"
        "ds_add_u64 v109, v[74:75] offset:32072\n"
        "s_mov_b64 exec, s[88:89]\n"
        "s_waitcnt lgkmcnt(4)\n"
        "v_cmp_le_u32_e32 vcc, s56, v112\n"
        "s_cbranch_vccz .Lg25_%=\n"
        "s_mov_b64 exec, vcc\n"
        "ds_add_rtn_u32 v112, v115, v114\n"
        "s_mov_b64 exec, s[88:89]\n"
        "s_waitcnt lgkmcnt(0)\n"
        ".Lg25_%=:\n"
        "v_and_b32 v112, 0x7ffff, v112\n"
        "ds_write_b64 v112, v[34:35]\n"
        "s_mov_b64 exec, s[90:91]\n"
        "s_waitcnt lgkmcnt(4)\n"
        "v_cmp_le_u32_e32 vcc, s56, v113\n"
        "s_cbranch_vccz .Lg26_%=\n"
        "s_mov_b64 exec, vcc\n"
        "ds_add_rtn_u32 v113, v115, v114\n"
        "s_mov_b64 exec, s[90:91]\n"
        "s_waitcnt lgkmcnt(0)\n"
        ".Lg26_%=:\n"
        "v_and_b32 v113, 0x7ffff, v113\n"
        "ds_write_b64 v113, v[36:37]\n"
        "s_mov_b64 exec, -1\n"
        "s_bitcmp1_b32 %[big], 2\n"
        "s_cbranch_scc0 .Lg27_%=\n"
        "s_waitcnt vmcnt(0) lgkmcnt(0)\n"
        "ds_read_b32 v111, v11 offset:416\n"
        "s_waitcnt lgkmcnt(0)\n"
        "v_readlane_b32 s54, v111, 12\n"
        "v_readlane_b32 s55, v111, 13\n"
        "s_nop 0\n"
        "v_mbcnt_lo_u32_b32 v110, s54, 0\n"
        "v_mbcnt_hi_u32_b32 v110, s55, v110\n"
        "s_bcnt1_i32_b64 s54, s[54:55]\n"
        "s_sub_u32 s54, 32, s54\n"
        "s_lshl2_add_u32 s52, s54, s53\n"
        "v_lshl_add_u32 v110, v110, 2, s52\n"
        "ds_read_b32 v110, v110 offset:480\n"
        "s_waitcnt lgkmcnt(0)\n"
        "v_add_u32 v110, v6, v110\n"
        "v_add_u32 v110, 3840, v110\n"
        "v_and_b32 v17, 2, v110\n"
        "v_and_b32 v110, -4, v110\n"
        "buffer_load_dwordx3 v[14:16], v110, s[36:39], 0 offen sc1\n"
        "v_cmp_ne_u32_e32 vcc, 0, v17\n"
        "s_waitcnt vmcnt(0)\n"
        "v_and_b32 v110, 0x7ffc0000, v15\n"
        "v_cmp_class_f32_e64 s[88:89], v15, %[cls]\n"
        "v_cndmask_b32_e32 v17, v14, v16, vcc\n"
        "v_mul_hi_u32 v110, v110, s47\n"
        "v_cndmask_b32_sdwa v16, v16, v14, vcc dst_sel:DWORD dst_unused:UNUSED_PAD src0_sel:WORD_0 src1_sel:WORD_1\n"
        "v_and_b32 v111, 0x7ffc0000, v17\n"
        "v_cmp_class_f32_e64 s[90:91], v17, %[cls]\n"
        "v_mul_hi_u32 v111, v111, s47\n"
        "v_and_b32 v110, -4, v110\n"
        "v_and_b32 v111, -4, v111\n"
        "s_mov_b64 exec, s[88:89]\n"
        "ds_add_rtn_u32 v112, v110, v114 offset:20016\n"
        "s_mov_b64 exec, s[90:91]\n"
        "ds_add_rtn_u32 v113, v111, v114 offset:20016\n"
        "s_mov_b64 exec, -1\n"
        "v_perm_b32 v14, v15, v16, s92\n"
        "v_perm_b32 v16, v17, v16, s93\n"
        "v_and_b32 v14, 0x7ff0000, v14\n"
        "v_and_b32 v16, 0x7ff0000, v16\n"
        "s_mov_b64 exec, s[88:89]\n"
        "s_waitcnt lgkmcnt(1)\n"
        "v_cmpx_le_u32_e32 vcc, s56, v112\n"
        "ds_add_rtn_u32 v112, v115, v114\n"
        "s_mov_b64 exec, s[90:91]\n"
        "s_waitcnt lgkmcnt(1)\n"
        "v_cmpx_le_u32_e32 vcc, s56, v113\n"
        "ds_add_rtn_u32 v113, v115, v114\n"
        "s_mov_b64 exec, -1\n"
        "s_nop 1\n"
        "s_mov_b64 exec, s[88:89]\n"
        "s_waitcnt lgkmcnt(1)\n"
        "v_and_b32 v112, 0x7ffff, v112\n"
        "ds_write_b64 v112, v[14:15]\n"
        "s_mov_b64 exec, s[90:91]\n"
        "s_waitcnt lgkmcnt(1)\n"
        "v_and_b32 v113, 0x7ffff, v113\n"
        "ds_write_b64 v113, v[16:17]\n"
        "s_mov_b64 exec, -1\n"
        "s_waitcnt lgkmcnt(0)\n"
        ".Lg27_%=:\n"
        "s_nop 1\n"
        "v_and_b32 v17, 2, v102\n"
        "v_and_b32 v102, -4, v102\n"
        "buffer_load_dwordx3 v[14:16], v102, s[36:39], 0 offen offset:0 sc1\n"
        "v_and_b32 v21, 2, v103\n"
        "v_and_b32 v103, -4, v103\n"
        "buffer_load_dwordx3 v[18:20], v103, s[36:39], 0 offen offset:640 sc1\n"
        "v_and_b32 v25, 2, v104\n"
        "v_and_b32 v104, -4, v104\n"
        "buffer_load_dwordx3 v[22:24], v104, s[36:39], 0 offen offset:1280 sc1\n"
        "v_and_b32 v29, 2, v105\n"
        "v_and_b32 v105, -4, v105\n"
        "buffer_load_dwordx3 v[26:28], v105, s[36:39], 0 offen offset:1920 sc1\n"
        "v_and_b32 v33, 2, v106\n"
        "v_and_b32 v106, -4, v106\n"
        "buffer_load_dwordx3 v[30:32], v106, s[36:39], 0 offen offset:2560 sc1\n"
        "v_and_b32 v37, 2, v107\n"
        "v_and_b32 v107, -4, v107\n"
        "buffer_load_dwordx3 v[34:36], v107, s[36:39], 0 offen offset:3200 sc1\n"
        "s_waitcnt lgkmcnt(0)\n"
        "s_barrier\n"
        "s_setprio 3\n"
        "s_add_u32 s40, s40, 640000\n"
        "s_addc_u32 s41, s41, 0\n"
        "s_sub_u32 s42, s42, 80000\n"
        "ds_read_b32 v110, v8\n"
        "ds_read_b64 v[58:59], v7 offset:0\n"
        "ds_read_b64 v[60:61], v7 offset:520\n"
        "ds_read_b64 v[62:63], v7 offset:1040\n"
        "ds_read_b64 v[64:65], v7 offset:1560\n"
        "ds_read_b64 v[38:39], v7 offset:2080\n"
        "ds_read_b64 v[40:41], v7 offset:2600\n"
        "ds_read_b64 v[42:43], v7 offset:3120\n"
        "ds_read_b64 v[44:45], v7 offset:3640\n"
        "ds_read_b64 v[46:47], v7 offset:4160\n"
        "v_subrev_u32 v111, %[ovd], v7\n"
        "ds_read_b64 v[48:49], v111 offset:4680\n"
        "v_subrev_u32 v111, %[ovd], v111\n"
        "ds_read_b64 v[50:51], v111 offset:5200\n"
        "v_subrev_u32 v111, %[ovd], v111\n"
        "ds_read_b64 v[52:53], v111 offset:5720\n"
        "ds_write_b32 v8, v9\n"
        "s_waitcnt lgkmcnt(13)\n"
        "v_and_b32 v110, 0xffffff, v110\n"
        "v_sub_u32 v110, v110, v10\n"
        "v_ashrrev_i32 v110, 3, v110\n"
        "v_add_u32 v12, v12, v110\n"
        "v_max_i32 v13, v13, v110\n"
        "v_med3_i32 v110, v110, 0, 63\n"
        "s_nop 0\n"
        "v_readlane_b32 s80, v110, 0\n"
        "v_readlane_b32 s82, v110, 1\n"
        "v_readlane_b32 s84, v110, 2\n"
        "v_readlane_b32 s86, v110, 3\n"
        "v_readlane_b32 s60, v110, 4\n"
        "v_readlane_b32 s62, v110, 5\n"
        "v_readlane_b32 s64, v110, 6\n"
        "v_readlane_b32 s66, v110, 7\n"
        "v_readlane_b32 s68, v110, 8\n"
        "v_readlane_b32 s70, v110, 9\n"
        "v_readlane_b32 s72, v110, 10\n"
        "v_readlane_b32 s74, v110, 11\n"
        "s_bfm_b64 s[80:81], s80, 0\n"
        "s_bfm_b64 s[82:83], s82, 0\n"
        "s_bfm_b64 s[84:85], s84, 0\n"
        "s_bfm_b64 s[86:87], s86, 0\n"
        "s_bfm_b64 s[60:61], s60, 0\n"
        "s_bfm_b64 s[62:63], s62, 0\n"
        "s_bfm_b64 s[64:65], s64, 0\n"
        "s_bfm_b64 s[66:67], s66, 0\n"
        "s_bfm_b64 s[68:69], s68, 0\n"
        "s_bfm_b64 s[70:71], s70, 0\n"
        "s_bfm_b64 s[72:73], s72, 0\n"
        "s_bfm_b64 s[74:75], s74, 0\n"
        "s_waitcnt lgkmcnt(12)\n"
        "v_bfe_u32 v101, v59, 14, 17\n"
        "v_pk_mul_f32 v[98:99], v[54:55], s[44:45] op_sel_hi:[0,1]\n"
        "v_mul_f32 v100, s46, v54\n"
        "v_mfma_f32_32x32x16_bf16 v[72:87], v[92:95], v[88:91], 0\n"
        "v_cos_f32 v98, v98\n"
        "v_cos_f32 v99, v99\n"
        "v_cos_f32 v100, v100\n"
        "s_mov_b64 exec, s[78:79]\n"
        "v_and_b32 v109, 0x3ff8, v57\n"
        "v_add_u32 v111, v109, v109\n"
        "ds_read_b128 v[88:91], v111 offset:0\n"
        "s_mov_b64 exec, s[80:81]\n"
        "buffer_load_dwordx2 v[66:67], v101, s[40:43], 0 idxen\n"
        "s_mov_b64 exec, -1\n"
        "s_waitcnt vmcnt(8)\n"
        "s_nop 0\n"
        "v_mfma_f32_4x4x4_16b_bf16 v[84:87], v[96:97], v[68:69], 0\n"
        "s_mov_b64 exec, s[76:77]\n"
        "v_pk_mul_f32 v[72:73], v[72:73], v[98:99] op_sel_hi:[1,0]\n"
        "v_pk_mul_f32 v[74:75], v[74:75], v[98:99] op_sel_hi:[1,0]\n"
        "v_pk_fma_f32 v[72:73], v[76:77], v[98:99], v[72:73] op_sel:[0,1,0] op_sel_hi:[1,1,1]\n"
        "v_pk_fma_f32 v[74:75], v[78:79], v[98:99], v[74:75] op_sel:[0,1,0] op_sel_hi:[1,1,1]\n"
        "v_pk_fma_f32 v[72:73], v[80:81], v[100:101], v[72:73] op_sel_hi:[1,0,1]\n"
        "v_pk_fma_f32 v[74:75], v[82:83], v[100:101], v[74:75] op_sel_hi:[1,0,1]\n"
        "v_pk_fma_f32 v[72:73], v[84:85], v[72:73], s[48:49]\n"
        "v_pk_fma_f32 v[74:75], v[86:87], v[74:75], s[50:51]\n"
        "ds_add_u64 v108, v[72:73] offset:22072\n"
        "ds_add_u64 v108, v[74:75] offset:32072\n"
        "s_mov_b64 exec, -1\n"
        "s_waitcnt lgkmcnt(3)\n"
        "s_barrier\n"
        "v_bfe_u32 v101, v61, 14, 17\n"
        "v_pk_mul_f32 v[98:99], v[56:57], s[44:45] op_sel_hi:[0,1]\n"
        "v_mul_f32 v100, s46, v56\n"
        "s_waitcnt lgkmcnt(2)\n"
        "v_mfma_f32_32x32x16_bf16 v[72:87], v[92:95], v[88:91], 0\n"
        "v_cos_f32 v98, v98\n"
        "v_cos_f32 v99, v99\n"
        "v_cos_f32 v100, v100\n"
        "s_mov_b64 exec, s[80:81]\n"
        "v_and_b32 v108, 0x3ff8, v59\n"
        "v_add_u32 v111, v108, v108\n"
        "ds_read_b128 v[88:91], v111 offset:0\n"
        "s_mov_b64 exec, s[82:83]\n"
        "buffer_load_dwordx2 v[68:69], v101, s[40:43], 0 idxen\n"
        "s_mov_b64 exec, -1\n"
        "s_waitcnt vmcnt(8)\n"
        "s_nop 0\n"
        "v_mfma_f32_4x4x4_16b_bf16 v[84:87], v[96:97], v[70:71], 0\n"
        "s_mov_b64 exec, s[78:79]\n"
        "v_pk_mul_f32 v[72:73], v[72:73], v[98:99] op_sel_hi:[1,0]\n"
        "v_pk_mul_f32 v[74:75], v[74:75], v[98:99] op_sel_hi:[1,0]\n"
        "v_pk_fma_f32 v[72:73], v[76:77], v[98:99], v[72:73] op_sel:[0,1,0] op_sel_hi:[1,1,1]\n"
        "v_pk_fma_f32 v[74:75], v[78:79], v[98:99], v[74:75] op_sel:[0,1,0] op_sel_hi:[1,1,1]\n"
        "v_pk_fma_f32 v[72:73], v[80:81], v[100:101], v[72:73] op_sel_hi:[1,0,1]\n"
        "v_pk_fma_f32 v[74:75], v[82:83], v[100:101], v[74:75] op_sel_hi:[1,0,1]\n"
        "v_pk_fma_f32 v[72:73], v[84:85], v[72:73], s[48:49]\n"
        "v_pk_fma_f32 v[74:75], v[86:87], v[74:75], s[50:51]\n"
        "ds_add_u64 v109, v[72:73] offset:22072\n"
        "ds_add_u64 v109, v[74:75] offset:32072\n"
        "s_mov_b64 exec, -1\n"
        "v_cmp_ne_u32_e32 vcc, 0, v17\n"
        "s_waitcnt vmcnt(7)\n"
        "v_and_b32 v110, 0x7ffc0000, v15\n"
        "v_cmp_class_f32_e64 s[88:89], v15, %[cls]\n"
        "v_cndmask_b32_e32 v17, v14, v16, vcc\n"
        "v_mul_hi_u32 v110, v110, s47\n"
        "v_cndmask_b32_sdwa v16, v16, v14, vcc dst_sel:DWORD dst_unused:UNUSED_PAD src0_sel:WORD_0 src1_sel:WORD_1\n"
        "v_and_b32 v111, 0x7ffc0000, v17\n"
        "v_cmp_class_f32_e64 s[90:91], v17, %[cls]\n"
        "v_mul_hi_u32 v111, v111, s47\n"
        "v_and_b32 v110, -4, v110\n"
        "v_and_b32 v111, -4, v111\n"
        "s_mov_b64 exec, s[88:89]\n"
        "ds_add_rtn_u32 v112, v110, v114 offset:20016\n"
        "s_mov_b64 exec, s[90:91]\n"
        "ds_add_rtn_u32 v113, v111, v114 offset:20016\n"
        "s_mov_b64 exec, -1\n"
        "v_perm_b32 v14, v15, v16, s92\n"
        "v_perm_b32 v16, v17, v16, s93\n"
        "v_and_b32 v14, 0x7ff0000, v14\n"
        "v_and_b32 v16, 0x7ff0000, v16\n"
        "v_bfe_u32 v101, v63, 14, 17\n"
        "v_pk_mul_f32 v[98:99], v[58:59], s[44:45] op_sel_hi:[0,1]\n"
        "v_mul_f32 v100, s46, v58\n"
        "s_waitcnt lgkmcnt(4)\n"
        "v_mfma_f32_32x32x16_bf16 v[72:87], v[92:95], v[88:91], 0\n"
        "v_cos_f32 v98, v98\n"
        "v_cos_f32 v99, v99\n"
        "v_cos_f32 v100, v100\n"
        "s_mov_b64 exec, s[82:83]\n"
        "v_and_b32 v109, 0x3ff8, v61\n"
        "v_add_u32 v111, v109, v109\n"
        "ds_read_b128 v[88:91], v111 offset:0\n"
        "s_mov_b64 exec, s[84:85]\n"
        "buffer_load_dwordx2 v[70:71], v101, s[40:43], 0 idxen\n"
        "s_mov_b64 exec, -1\n"
        "s_waitcnt vmcnt(2)\n"
        "s_nop 0\n"
        "v_mfma_f32_4x4x4_16b_bf16 v[84:87], v[96:97], v[66:67], 0\n"
        "s_mov_b64 exec, s[80:81]\n"
        "v_pk_mul_f32 v[72:73], v[72:73], v[98:99] op_sel_hi:[1,0]\n"
        "v_pk_mul_f32 v[74:75], v[74:75], v[98:99] op_sel_hi:[1,0]\n"
        "v_pk_fma_f32 v[72:73], v[76:77], v[98:99], v[72:73] op_sel:[0,1,0] op_sel_hi:[1,1,1]\n"
        "v_pk_fma_f32 v[74:75], v[78:79], v[98:99], v[74:75] op_sel:[0,1,0] op_sel_hi:[1,1,1]\n"
        "v_pk_fma_f32 v[72:73], v[80:81], v[100:101], v[72:73] op_sel_hi:[1,0,1]\n"
        "v_pk_fma_f32 v[74:75], v[82:83], v[100:101], v[74:75] op_sel_hi:[1,0,1]\n"
        "v_pk_fma_f32 v[72:73], v[84:85], v[72:73], s[48:49]\n"
        "v_pk_fma_f32 v[74:75], v[86:87], v[74:75], s[50:51]\n"
        "ds_add_u64 v108, v[72:73] offset:22072\n"
        "ds_add_u64 v108, v[74:75] offset:32072\n"
        "s_mov_b64 exec, s[88:89]\n"
        "s_waitcnt lgkmcnt(4)\n"
        "v_cmp_le_u32_e32 vcc, s56, v112\n"
        "s_cbranch_vccz .Lg28_%=\n"
        "s_mov_b64 exec, vcc\n"
        "ds_add_rtn_u32 v112, v115, v114\n"
        "s_mov_b64 exec, s[88:89]\n"
        "s_waitcnt lgkmcnt(0)\n"
        ".Lg28_%=:\n"
        "v_and_b32 v112, 0x7ffff, v112\n"
        "ds_write_b64 v112, v[14:15]\n"
        "s_mov_b64 exec, s[90:91]\n"
        "s_waitcnt lgkmcnt(4)\n"
        "v_cmp_le_u32_e32 vcc, s56, v113\n"
        "s_cbranch_vccz .Lg29_%=\n"
        "s_mov_b64 exec, vcc\n"
        "ds_add_rtn_u32 v113, v115, v114\n"
        "s_mov_b64 exec, s[90:91]\n"
        "s_waitcnt lgkmcnt(0)\n"
        ".Lg29_%=:\n"
        "v_and_b32 v113, 0x7ffff, v113\n"
        "ds_write_b64 v113, v[16:17]\n"
        "s_mov_b64 exec, -1\n"
        "s_setprio 2\n"
        "v_bfe_u32 v101, v65, 14, 17\n"
        "v_pk_mul_f32 v[98:99], v[60:61], s[44:45] op_sel_hi:[0,1]\n"
        "v_mul_f32 v100, s46, v60\n"
        "s_waitcnt lgkmcnt(4)\n"
        "v_mfma_f32_32x32x16_bf16 v[72:87], v[92:95], v[88:91], 0\n"
        "v_cos_f32 v98, v98\n"
        "v_cos_f32 v99, v99\n"
        "v_cos_f32 v100, v100\n"
        "s_mov_b64 exec, s[84:85]\n"
        "v_and_b32 v108, 0x3ff8, v63\n"
        "v_add_u32 v111, v108, v108\n"
        "ds_read_b128 v[88:91], v111 offset:0\n"
        "s_mov_b64 exec, s[86:87]\n"
        "buffer_load_dwordx2 v[66:67], v101, s[40:43], 0 idxen\n"
        "s_mov_b64 exec, -1\n"
        "s_waitcnt vmcnt(2)\n"
        "s_nop 0\n"
        "v_mfma_f32_4x4x4_16b_bf16 v[84:87], v[96:97], v[68:69], 0\n"
        "s_mov_b64 exec, s[82:83]\n"
        "v_pk_mul_f32 v[72:73], v[72:73], v[98:99] op_sel_hi:[1,0]\n"
        "v_pk_mul_f32 v[74:75], v[74:75], v[98:99] op_sel_hi:[1,0]\n"
        "v_pk_fma_f32 v[72:73], v[76:77], v[98:99], v[72:73] op_sel:[0,1,0] op_sel_hi:[1,1,1]\n"
        "v_pk_fma_f32 v[74:75], v[78:79], v[98:99], v[74:75] op_sel:[0,1,0] op_sel_hi:[1,1,1]\n"
        "v_pk_fma_f32 v[72:73], v[80:81], v[100:101], v[72:73] op_sel_hi:[1,0,1]\n"
        "v_pk_fma_f32 v[74:75], v[82:83], v[100:101], v[74:75] op_sel_hi:[1,0,1]\n"
        "v_pk_fma_f32 v[72:73], v[84:85], v[72:73], s[48:49]\n"
        "v_pk_fma_f32 v[74:75], v[86:87], v[74:75], s[50:51]\n"
        "ds_add_u64 v109, v[72:73] offset:22072\n"
        "ds_add_u64 v109, v[74:75] offset:32072\n"
        "s_mov_b64 exec, -1\n"
        "v_cmp_ne_u32_e32 vcc, 0, v21\n"
        "v_and_b32 v110, 0x7ffc0000, v19\n"
        "v_cmp_class_f32_e64 s[88:89], v19, %[cls]\n"
        "v_cndmask_b32_e32 v21, v18, v20, vcc\n"
        "v_mul_hi_u32 v110, v110, s47\n"
        "v_cndmask_b32_sdwa v20, v20, v18, vcc dst_sel:DWORD dst_unused:UNUSED_PAD src0_sel:WORD_0 src1_sel:WORD_1\n"
        "v_and_b32 v111, 0x7ffc0000, v21\n"
        "v_cmp_class_f32_e64 s[90:91], v21, %[cls]\n"
        "v_mul_hi_u32 v111, v111, s47\n"
        "v_and_b32 v110, -4, v110\n"
        "v_and_b32 v111, -4, v111\n"
        "s_mov_b64 exec, s[88:89]\n"
        "ds_add_rtn_u32 v112, v110, v114 offset:20016\n"
        "s_mov_b64 exec, s[90:91]\n"
        "ds_add_rtn_u32 v113, v111, v114 offset:20016\n"
        "s_mov_b64 exec, -1\n"
        "v_perm_b32 v18, v19, v20, s92\n"
        "v_perm_b32 v20, v21, v20, s93\n"
        "v_and_b32 v18, 0x7ff0000, v18\n"
        "v_and_b32 v20, 0x7ff0000, v20\n"
        "v_bfe_u32 v101, v39, 14, 17\n"
        "v_pk_mul_f32 v[98:99], v[62:63], s[44:45] op_sel_hi:[0,1]\n"
        "v_mul_f32 v100, s46, v62\n"
        "s_waitcnt lgkmcnt(4)\n"
        "v_mfma_f32_32x32x16_bf16 v[72:87], v[92:95], v[88:91], 0\n"
        "v_cos_f32 v98, v98\n"
        "v_cos_f32 v99, v99\n"
        "v_cos_f32 v100, v100\n"
        "s_mov_b64 exec, s[86:87]\n"
        "v_and_b32 v109, 0x3ff8, v65\n"
        "v_add_u32 v111, v109, v109\n"
        "ds_read_b128 v[88:91], v111 offset:0\n"
        "s_mov_b64 exec, s[60:61]\n"
        "buffer_load_dwordx2 v[68:69], v101, s[40:43], 0 idxen\n"
        "s_mov_b64 exec, -1\n"
        "s_waitcnt vmcnt(2)\n"
        "s_nop 0\n"
        "v_mfma_f32_4x4x4_16b_bf16 v[84:87], v[96:97], v[70:71], 0\n"
        "s_mov_b64 exec, s[84:85]\n"
        "v_pk_mul_f32 v[72:73], v[72:73], v[98:99] op_sel_hi:[1,0]\n"
        "v_pk_mul_f32 v[74:75], v[74:75], v[98:99] op_sel_hi:[1,0]\n"
        "v_pk_fma_f32 v[72:73], v[76:77], v[98:99], v[72:73] op_sel:[0,1,0] op_sel_hi:[1,1,1]\n"
        "v_pk_fma_f32 v[74:75], v[78:79], v[98:99], v[74:75] op_sel:[0,1,0] op_sel_hi:[1,1,1]\n"
        "v_pk_fma_f32 v[72:73], v[80:81], v[100:101], v[72:73] op_sel_hi:[1,0,1]\n"
        "v_pk_fma_f32 v[74:75], v[82:83], v[100:101], v[74:75] op_sel_hi:[1,0,1]\n"
        "v_pk_fma_f32 v[72:73], v[84:85], v[72:73], s[48:49]\n"
        "v_pk_fma_f32 v[74:75], v[86:87], v[74:75], s[50:51]\n"
        "ds_add_u64 v108, v[72:73] offset:22072\n"
        "ds_add_u64 v108, v[74:75] offset:32072\n"
        "s_mov_b64 exec, s[88:89]\n"
        "s_waitcnt lgkmcnt(4)\n"
        "v_cmp_le_u32_e32 vcc, s56, v112\n"
        "s_cbranch_vccz .Lg30_%=\n"
        "s_mov_b64 exec, vcc\n"
        "ds_add_rtn_u32 v112, v115, v114\n"
        "s_mov_b64 exec, s[88:89]\n"
        "s_waitcnt lgkmcnt(0)\n"
        ".Lg30_%=:\n"
        "v_and_b32 v112, 0x7ffff, v112\n"
        "ds_write_b64 v112, v[18:19]\n"
        "s_mov_b64 exec, s[90:91]\n"
        "s_waitcnt lgkmcnt(4)\n"
        "v_cmp_le_u32_e32 vcc, s56, v113\n"
        "s_cbranch_vccz .Lg31_%=\n"
        "s_mov_b64 exec, vcc\n"
        "ds_add_rtn_u32 v113, v115, v114\n"
        "s_mov_b64 exec, s[90:91]\n"
        "s_waitcnt lgkmcnt(0)\n"
        ".Lg31_%=:\n"
        "v_and_b32 v113, 0x7ffff, v113\n"
        "ds_write_b64 v113, v[20:21]\n"
        "s_mov_b64 exec, -1\n"
        "v_bfe_u32 v101, v41, 14, 17\n"
        "v_pk_mul_f32 v[98:99], v[64:65], s[44:45] op_sel_hi:[0,1]\n"
        "v_mul_f32 v100, s46, v64\n"
        "s_waitcnt lgkmcnt(4)\n"
        "v_mfma_f32_32x32x16_bf16 v[72:87], v[92:95], v[88:91], 0\n"
        "v_cos_f32 v98, v98\n"
        "v_cos_f32 v99, v99\n"
        "v_cos_f32 v100, v100\n"
        "s_mov_b64 exec, s[60:61]\n"
        "v_and_b32 v108, 0x3ff8, v39\n"
        "v_add_u32 v111, v108, v108\n"
        "ds_read_b128 v[88:91], v111 offset:0\n"
        "s_mov_b64 exec, s[62:63]\n"
        "buffer_load_dwordx2 v[70:71], v101, s[40:43], 0 idxen\n"
        "s_mov_b64 exec, -1\n"
        "s_waitcnt vmcnt(2)\n"
        "s_nop 0\n"
        "v_mfma_f32_4x4x4_16b_bf16 v[84:87], v[96:97], v[66:67], 0\n"
        "s_mov_b64 exec, s[86:87]\n"
        "v_pk_mul_f32 v[72:73], v[72:73], v[98:99] op_sel_hi:[1,0]\n"
        "v_pk_mul_f32 v[74:75], v[74:75], v[98:99] op_sel_hi:[1,0]\n"
        "v_pk_fma_f32 v[72:73], v[76:77], v[98:99], v[72:73] op_sel:[0,1,0] op_sel_hi:[1,1,1]\n"
        "v_pk_fma_f32 v[74:75], v[78:79], v[98:99], v[74:75] op_sel:[0,1,0] op_sel_hi:[1,1,1]\n"
        "v_pk_fma_f32 v[72:73], v[80:81], v[100:101], v[72:73] op_sel_hi:[1,0,1]\n"
        "v_pk_fma_f32 v[74:75], v[82:83], v[100:101], v[74:75] op_sel_hi:[1,0,1]\n"
        "v_pk_fma_f32 v[72:73], v[84:85], v[72:73], s[48:49]\n"
        "v_pk_fma_f32 v[74:75], v[86:87], v[74:75], s[50:51]\n"
        "ds_add_u64 v109, v[72:73] offset:22072\n"
        "ds_add_u64 v109, v[74:75] offset:32072\n"
        "s_mov_b64 exec, -1\n"
        "v_cmp_ne_u32_e32 vcc, 0, v25\n"
        "v_and_b32 v110, 0x7ffc0000, v23\n"
        "v_cmp_class_f32_e64 s[88:89], v23, %[cls]\n"
        "v_cndmask_b32_e32 v25, v22, v24, vcc\n"
        "v_mul_hi_u32 v110, v110, s47\n"
        "v_cndmask_b32_sdwa v24, v24, v22, vcc dst_sel:DWORD dst_unused:UNUSED_PAD src0_sel:WORD_0 src1_sel:WORD_1\n"
        "v_and_b32 v111, 0x7ffc0000, v25\n"
        "v_cmp_class_f32_e64 s[90:91], v25, %[cls]\n"
        "v_mul_hi_u32 v111, v111, s47\n"
        "v_and_b32 v110, -4, v110\n"
        "v_and_b32 v111, -4, v111\n"
        "s_mov_b64 exec, s[88:89]\n"
        "ds_add_rtn_u32 v112, v110, v114 offset:20016\n"
        "s_mov_b64 exec, s[90:91]\n"
        "ds_add_rtn_u32 v113, v111, v114 offset:20016\n"
        "s_mov_b64 exec, -1\n"
        "v_perm_b32 v22, v23, v24, s92\n"
        "v_perm_b32 v24, v25, v24, s93\n"
        "v_and_b32 v22, 0x7ff0000, v22\n"
        "v_and_b32 v24, 0x7ff0000, v24\n"
        "s_setprio 1\n"
        "v_bfe_u32 v101, v43, 14, 17\n"
        "v_pk_mul_f32 v[98:99], v[38:39], s[44:45] op_sel_hi:[0,1]\n"
        "v_mul_f32 v100, s46, v38\n"
        "s_waitcnt lgkmcnt(4)\n"
        "v_mfma_f32_32x32x16_bf16 v[72:87], v[92:95], v[88:91], 0\n"
        "v_cos_f32 v98, v98\n"
        "v_cos_f32 v99, v99\n"
        "v_cos_f32 v100, v100\n"
        "s_mov_b64 exec, s[62:63]\n"
        "v_and_b32 v109, 0x3ff8, v41\n"
        "v_add_u32 v111, v109, v109\n"
        "ds_read_b128 v[88:91], v111 offset:0\n"
        "s_mov_b64 exec, s[64:65]\n"
        "buffer_load_dwordx2 v[66:67], v101, s[40:43], 0 idxen\n"
        "s_mov_b64 exec, -1\n"
        "s_waitcnt vmcnt(2)\n"
        "s_nop 0\n"
        "v_mfma_f32_4x4x4_16b_bf16 v[84:87], v[96:97], v[68:69], 0\n"
        "s_mov_b64 exec, s[60:61]\n"
        "v_pk_mul_f32 v[72:73], v[72:73], v[98:99] op_sel_hi:[1,0]\n"
        "v_pk_mul_f32 v[74:75], v[74:75], v[98:99] op_sel_hi:[1,0]\n"
        "v_pk_fma_f32 v[72:73], v[76:77], v[98:99], v[72:73] op_sel:[0,1,0] op_sel_hi:[1,1,1]\n"
        "v_pk_fma_f32 v[74:75], v[78:79], v[98:99], v[74:75] op_sel:[0,1,0] op_sel_hi:[1,1,1]\n"
        "v_pk_fma_f32 v[72:73], v[80:81], v[100:101], v[72:73] op_sel_hi:[1,0,1]\n"
        "v_pk_fma_f32 v[74:75], v[82:83], v[100:101], v[74:75] op_sel_hi:[1,0,1]\n"
        "v_pk_fma_f32 v[72:73], v[84:85], v[72:73], s[48:49]\n"
        "v_pk_fma_f32 v[74:75], v[86:87], v[74:75], s[50:51]\n"
        "ds_add_u64 v108, v[72:73] offset:22072\n"
        "ds_add_u64 v108, v[74:75] offset:32072\n"
        "s_mov_b64 exec, s[88:89]\n"
        "s_waitcnt lgkmcnt(4)\n"
        "v_cmp_le_u32_e32 vcc, s56, v112\n"
        "s_cbranch_vccz .Lg32_%=\n"
        "s_mov_b64 exec, vcc\n"
        "ds_add_rtn_u32 v112, v115, v114\n"
        "s_mov_b64 exec, s[88:89]\n"
        "s_waitcnt lgkmcnt(0)\n"
        ".Lg32_%=:\n"
        "v_and_b32 v112, 0x7ffff, v112\n"
        "ds_write_b64 v112, v[22:23]\n"
        "s_mov_b64 exec, s[90:91]\n"
        "s_waitcnt lgkmcnt(4)\n"
        "v_cmp_le_u32_e32 vcc, s56, v113\n"
        "s_cbranch_vccz .Lg33_%=\n"
        "s_mov_b64 exec, vcc\n"
        "ds_add_rtn_u32 v113, v115, v114\n"
        "s_mov_b64 exec, s[90:91]\n"
        "s_waitcnt lgkmcnt(0)\n"
        ".Lg33_%=:\n"
        "v_and_b32 v113, 0x7ffff, v113\n"
        "ds_write_b64 v113, v[24:25]\n"
        "s_mov_b64 exec, -1\n"
        "v_bfe_u32 v101, v45, 14, 17\n"
        "v_pk_mul_f32 v[98:99], v[40:41], s[44:45] op_sel_hi:[0,1]\n"
        "v_mul_f32 v100, s46, v40\n"
        "s_waitcnt lgkmcnt(4)\n"
        "v_mfma_f32_32x32x16_bf16 v[72:87], v[92:95], v[88:91], 0\n"
        "v_cos_f32 v98, v98\n"
        "v_cos_f32 v99, v99\n"
        "v_cos_f32 v100, v100\n"
        "s_mov_b64 exec, s[64:65]\n"
        "v_and_b32 v108, 0x3ff8, v43\n"
        "v_add_u32 v111, v108, v108\n"
        "ds_read_b128 v[88:91], v111 offset:0\n"
        "s_mov_b64 exec, s[66:67]\n"
        "buffer_load_dwordx2 v[68:69], v101, s[40:43], 0 idxen\n"
        "s_mov_b64 exec, -1\n"
        "s_waitcnt vmcnt(2)\n"
        "s_nop 0\n"
        "v_mfma_f32_4x4x4_16b_bf16 v[84:87], v[96:97], v[70:71], 0\n"
        "s_mov_b64 exec, s[62:63]\n"
        "v_pk_mul_f32 v[72:73], v[72:73], v[98:99] op_sel_hi:[1,0]\n"
        "v_pk_mul_f32 v[74:75], v[74:75], v[98:99] op_sel_hi:[1,0]\n"
        "v_pk_fma_f32 v[72:73], v[76:77], v[98:99], v[72:73] op_sel:[0,1,0] op_sel_hi:[1,1,1]\n"
        "v_pk_fma_f32 v[74:75], v[78:79], v[98:99], v[74:75] op_sel:[0,1,0] op_sel_hi:[1,1,1]\n"
        "v_pk_fma_f32 v[72:73], v[80:81], v[100:101], v[72:73] op_sel_hi:[1,0,1]\n"
        "v_pk_fma_f32 v[74:75], v[82:83], v[100:101], v[74:75] op_sel_hi:[1,0,1]\n"
        "v_pk_fma_f32 v[72:73], v[84:85], v[72:73], s[48:49]\n"
        "v_pk_fma_f32 v[74:75], v[86:87], v[74:75], s[50:51]\n"
        "ds_add_u64 v109, v[72:73] offset:22072\n"
        "ds_add_u64 v109, v[74:75] offset:32072\n"
        "s_mov_b64 exec, -1\n"
        "v_cmp_ne_u32_e32 vcc, 0, v29\n"
        "v_and_b32 v110, 0x7ffc0000, v27\n"
        "v_cmp_class_f32_e64 s[88:89], v27, %[cls]\n"
        "v_cndmask_b32_e32 v29, v26, v28, vcc\n"
        "v_mul_hi_u32 v110, v110, s47\n"
        "v_cndmask_b32_sdwa v28, v28, v26, vcc dst_sel:DWORD dst_unused:UNUSED_PAD src0_sel:WORD_0 src1_sel:WORD_1\n"
        "v_and_b32 v111, 0x7ffc0000, v29\n"
        "v_cmp_class_f32_e64 s[90:91], v29, %[cls]\n"
        "v_mul_hi_u32 v111, v111, s47\n"
        "v_and_b32 v110, -4, v110\n"
        "v_and_b32 v111, -4, v111\n"
        "s_mov_b64 exec, s[88:89]\n"
        "ds_add_rtn_u32 v112, v110, v114 offset:20016\n"
        "s_mov_b64 exec, s[90:91]\n"
        "ds_add_rtn_u32 v113, v111, v114 offset:20016\n"
        "s_mov_b64 exec, -1\n"
        "v_perm_b32 v26, v27, v28, s92\n"
        "v_perm_b32 v28, v29, v28, s93\n"
        "v_and_b32 v26, 0x7ff0000, v26\n"
        "v_and_b32 v28, 0x7ff0000, v28\n"
        "v_bfe_u32 v101, v47, 14, 17\n"
        "v_pk_mul_f32 v[98:99], v[42:43], s[44:45] op_sel_hi:[0,1]\n"
        "v_mul_f32 v100, s46, v42\n"
        "s_waitcnt lgkmcnt(4)\n"
        "v_mfma_f32_32x32x16_bf16 v[72:87], v[92:95], v[88:91], 0\n"
        "v_cos_f32 v98, v98\n"
        "v_cos_f32 v99, v99\n"
        "v_cos_f32 v100, v100\n"
        "s_mov_b64 exec, s[66:67]\n"
        "v_and_b32 v109, 0x3ff8, v45\n"
        "v_add_u32 v111, v109, v109\n"
        "ds_read_b128 v[88:91], v111 offset:0\n"
        "s_mov_b64 exec, s[68:69]\n"
        "buffer_load_dwordx2 v[70:71], v101, s[40:43], 0 idxen\n"
        "s_mov_b64 exec, -1\n"
        "s_waitcnt vmcnt(2)\n"
        "s_nop 0\n"
        "v_mfma_f32_4x4x4_16b_bf16 v[84:87], v[96:97], v[66:67], 0\n"
        "s_mov_b64 exec, s[64:65]\n"
        "v_pk_mul_f32 v[72:73], v[72:73], v[98:99] op_sel_hi:[1,0]\n"
        "v_pk_mul_f32 v[74:75], v[74:75], v[98:99] op_sel_hi:[1,0]\n"
        "v_pk_fma_f32 v[72:73], v[76:77], v[98:99], v[72:73] op_sel:[0,1,0] op_sel_hi:[1,1,1]\n"
        "v_pk_fma_f32 v[74:75], v[78:79], v[98:99], v[74:75] op_sel:[0,1,0] op_sel_hi:[1,1,1]\n"
        "v_pk_fma_f32 v[72:73], v[80:81], v[100:101], v[72:73] op_sel_hi:[1,0,1]\n"
        "v_pk_fma_f32 v[74:75], v[82:83], v[100:101], v[74:75] op_sel_hi:[1,0,1]\n"
        "v_pk_fma_f32 v[72:73], v[84:85], v[72:73], s[48:49]\n"
        "v_pk_fma_f32 v[74:75], v[86:87], v[74:75], s[50:51]\n"
        "ds_add_u64 v108, v[72:73] offset:22072\n"
        "ds_add_u64 v108, v[74:75] offset:32072\n"
        "s_mov_b64 exec, s[88:89]\n"
        "s_waitcnt lgkmcnt(4)\n"
        "v_cmp_le_u32_e32 vcc, s56, v112\n"
        "s_cbranch_vccz .Lg34_%=\n"
        "s_mov_b64 exec, vcc\n"
        "ds_add_rtn_u32 v112, v115, v114\n"
        "s_mov_b64 exec, s[88:89]\n"
        "s_waitcnt lgkmcnt(0)\n"
        ".Lg34_%=:\n"
        "v_and_b32 v112, 0x7ffff, v112\n"
        "ds_write_b64 v112, v[26:27]\n"
        "s_mov_b64 exec, s[90:91]\n"
        "s_waitcnt lgkmcnt(4)\n"
        "v_cmp_le_u32_e32 vcc, s56, v113\n"
        "s_cbranch_vccz .Lg35_%=\n"
        "s_mov_b64 exec, vcc\n"
        "ds_add_rtn_u32 v113, v115, v114\n"
        "s_mov_b64 exec, s[90:91]\n"
        "s_waitcnt lgkmcnt(0)\n"
        ".Lg35_%=:\n"
        "v_and_b32 v113, 0x7ffff, v113\n"
        "ds_write_b64 v113, v[28:29]\n"
        "s_mov_b64 exec, -1\n"
        "s_setprio 0\n"
        "v_bfe_u32 v101, v49, 14, 17\n"
        "v_pk_mul_f32 v[98:99], v[44:45], s[44:45] op_sel_hi:[0,1]\n"
        "v_mul_f32 v100, s46, v44\n"
        "s_waitcnt lgkmcnt(4)\n"
        "v_mfma_f32_32x32x16_bf16 v[72:87], v[92:95], v[88:91], 0\n"
        "v_cos_f32 v98, v98\n"
        "v_cos_f32 v99, v99\n"
        "v_cos_f32 v100, v100\n"
        "s_mov_b64 exec, s[68:69]\n"
        "v_and_b32 v108, 0x3ff8, v47\n"
        "v_add_u32 v111, v108, v108\n"
        "ds_read_b128 v[88:91], v111 offset:0\n"
        "s_mov_b64 exec, s[70:71]\n"
        "buffer_load_dwordx2 v[66:67], v101, s[40:43], 0 idxen\n"
        "s_mov_b64 exec, -1\n"
        "s_waitcnt vmcnt(2)\n"
        "s_nop 0\n"
        "v_mfma_f32_4x4x4_16b_bf16 v[84:87], v[96:97], v[68:69], 0\n"
        "s_mov_b64 exec, s[66:67]\n"
        "v_pk_mul_f32 v[72:73], v[72:73], v[98:99] op_sel_hi:[1,0]\n"
        "v_pk_mul_f32 v[74:75], v[74:75], v[98:99] op_sel_hi:[1,0]\n"
        "v_pk_fma_f32 v[72:73], v[76:77], v[98:99], v[72:73] op_sel:[0,1,0] op_sel_hi:[1,1,1]\n"
        "v_pk_fma_f32 v[74:75], v[78:79], v[98:99], v[74:75] op_sel:[0,1,0] op_sel_hi:[1,1,1]\n"
        "v_pk_fma_f32 v[72:73], v[80:81], v[100:101], v[72:73] op_sel_hi:[1,0,1]\n"
        "v_pk_fma_f32 v[74:75], v[82:83], v[100:101], v[74:75] op_sel_hi:[1,0,1]\n"
        "v_pk_fma_f32 v[72:73], v[84:85], v[72:73], s[48:49]\n"
        "v_pk_fma_f32 v[74:75], v[86:87], v[74:75], s[50:51]\n"
        "ds_add_u64 v109, v[72:73] offset:22072\n"
        "ds_add_u64 v109, v[74:75] offset:32072\n"
        "s_mov_b64 exec, -1\n"
        "v_cmp_ne_u32_e32 vcc, 0, v33\n"
        "v_and_b32 v110, 0x7ffc0000, v31\n"
        "v_cmp_class_f32_e64 s[88:89], v31, %[cls]\n"
        "v_cndmask_b32_e32 v33, v30, v32, vcc\n"
        "v_mul_hi_u32 v110, v110, s47\n"
        "v_cndmask_b32_sdwa v32, v32, v30, vcc dst_sel:DWORD dst_unused:UNUSED_PAD src0_sel:WORD_0 src1_sel:WORD_1\n"
        "v_and_b32 v111, 0x7ffc0000, v33\n"
        "v_cmp_class_f32_e64 s[90:91], v33, %[cls]\n"
        "v_mul_hi_u32 v111, v111, s47\n"
        "v_and_b32 v110, -4, v110\n"
        "v_and_b32 v111, -4, v111\n"
        "s_mov_b64 exec, s[88:89]\n"
        "ds_add_rtn_u32 v112, v110, v114 offset:20016\n"
        "s_mov_b64 exec, s[90:91]\n"
        "ds_add_rtn_u32 v113, v111, v114 offset:20016\n"
        "s_mov_b64 exec, -1\n"
        "v_perm_b32 v30, v31, v32, s92\n"
        "v_perm_b32 v32, v33, v32, s93\n"
        "v_and_b32 v30, 0x7ff0000, v30\n"
        "v_and_b32 v32, 0x7ff0000, v32\n"
        "v_bfe_u32 v101, v51, 14, 17\n"
        "v_pk_mul_f32 v[98:99], v[46:47], s[44:45] op_sel_hi:[0,1]\n"
        "v_mul_f32 v100, s46, v46\n"
        "s_waitcnt lgkmcnt(4)\n"
        "v_mfma_f32_32x32x16_bf16 v[72:87], v[92:95], v[88:91], 0\n"
        "v_cos_f32 v98, v98\n"
        "v_cos_f32 v99, v99\n"
        "v_cos_f32 v100, v100\n"
        "s_mov_b64 exec, s[70:71]\n"
        "v_and_b32 v109, 0x3ff8, v49\n"
        "v_add_u32 v111, v109, v109\n"
        "ds_read_b128 v[88:91], v111 offset:0\n"
        "s_mov_b64 exec, s[72:73]\n"
        "buffer_load_dwordx2 v[68:69], v101, s[40:43], 0 idxen\n"
        "s_mov_b64 exec, -1\n"
        "s_waitcnt vmcnt(2)\n"
        "s_nop 0\n"
        "v_mfma_f32_4x4x4_16b_bf16 v[84:87], v[96:97], v[70:71], 0\n"
        "s_mov_b64 exec, s[68:69]\n"
        "v_pk_mul_f32 v[72:73], v[72:73], v[98:99] op_sel_hi:[1,0]\n"
        "v_pk_mul_f32 v[74:75], v[74:75], v[98:99] op_sel_hi:[1,0]\n"
        "v_pk_fma_f32 v[72:73], v[76:77], v[98:99], v[72:73] op_sel:[0,1,0] op_sel_hi:[1,1,1]\n"
        "v_pk_fma_f32 v[74:75], v[78:79], v[98:99], v[74:75] op_sel:[0,1,0] op_sel_hi:[1,1,1]\n"
        "v_pk_fma_f32 v[72:73], v[80:81], v[100:101], v[72:73] op_sel_hi:[1,0,1]\n"
        "v_pk_fma_f32 v[74:75], v[82:83], v[100:101], v[74:75] op_sel_hi:[1,0,1]\n"
        "v_pk_fma_f32 v[72:73], v[84:85], v[72:73], s[48:49]\n"
        "v_pk_fma_f32 v[74:75], v[86:87], v[74:75], s[50:51]\n"
        "ds_add_u64 v108, v[72:73] offset:22072\n"
        "ds_add_u64 v108, v[74:75] offset:32072\n"
        "s_mov_b64 exec, s[88:89]\n"
        "s_waitcnt lgkmcnt(4)\n"
        "v_cmp_le_u32_e32 vcc, s56, v112\n"
        "s_cbranch_vccz .Lg36_%=\n"
        "s_mov_b64 exec, vcc\n"
        "ds_add_rtn_u32 v112, v115, v114\n"
        "s_mov_b64 exec, s[88:89]\n"
        "s_waitcnt lgkmcnt(0)\n"
        ".Lg36_%=:\n"
        "v_and_b32 v112, 0x7ffff, v112\n"
        "ds_write_b64 v112, v[30:31]\n"
        "s_mov_b64 exec, s[90:91]\n"
        "s_waitcnt lgkmcnt(4)\n"
        "v_cmp_le_u32_e32 vcc, s56, v113\n"
        "s_cbranch_vccz .Lg37_%=\n"
        "s_mov_b64 exec, vcc\n"
        "ds_add_rtn_u32 v113, v115, v114\n"
        "s_mov_b64 exec, s[90:91]\n"
        "s_waitcnt lgkmcnt(0)\n"
        ".Lg37_%=:\n"
        "v_and_b32 v113, 0x7ffff, v113\n"
        "ds_write_b64 v113, v[32:33]\n"
        "s_mov_b64 exec, -1\n"
        "v_cmp_ne_u32_e32 vcc, 0, v37\n"
        "v_and_b32 v110, 0x7ffc0000, v35\n"
        "v_cmp_class_f32_e64 s[88:89], v35, %[cls]\n"
        "v_cndmask_b32_e32 v37, v34, v36, vcc\n"
        "v_mul_hi_u32 v110, v110, s47\n"
        "v_cndmask_b32_sdwa v36, v36, v34, vcc dst_sel:DWORD dst_unused:UNUSED_PAD src0_sel:WORD_0 src1_sel:WORD_1\n"
        "v_and_b32 v111, 0x7ffc0000, v37\n"
        "v_cmp_class_f32_e64 s[90:91], v37, %[cls]\n"
        "v_mul_hi_u32 v111, v111, s47\n"
        "v_and_b32 v110, -4, v110\n"
        "v_and_b32 v111, -4, v111\n"
        "s_mov_b64 exec, s[88:89]\n"
        "ds_add_rtn_u32 v112, v110, v114 offset:20016\n"
        "s_mov_b64 exec, s[90:91]\n"
        "ds_add_rtn_u32 v113, v111, v114 offset:20016\n"
        "s_mov_b64 exec, -1\n"
        "v_perm_b32 v34, v35, v36, s92\n"
        "v_perm_b32 v36, v37, v36, s93\n"
        "v_and_b32 v34, 0x7ff0000, v34\n"
        "v_and_b32 v36, 0x7ff0000, v36\n"
        "v_bfe_u32 v101, v53, 14, 17\n"
        "v_pk_mul_f32 v[98:99], v[48:49], s[44:45] op_sel_hi:[0,1]\n"
        "v_mul_f32 v100, s46, v48\n"
        "s_waitcnt lgkmcnt(6)\n"
        "v_mfma_f32_32x32x16_bf16 v[72:87], v[92:95], v[88:91], 0\n"
        "v_cos_f32 v98, v98\n"
        "v_cos_f32 v99, v99\n"
        "v_cos_f32 v100, v100\n"
        "s_mov_b64 exec, s[72:73]\n"
        "v_and_b32 v108, 0x3ff8, v51\n"
        "v_add_u32 v111, v108, v108\n"
        "ds_read_b128 v[88:91], v111 offset:0\n"
        "s_mov_b64 exec, s[74:75]\n"
        "buffer_load_dwordx2 v[70:71], v101, s[40:43], 0 idxen\n"
        "s_mov_b64 exec, -1\n"
        "s_waitcnt vmcnt(2)\n"
        "s_nop 0\n"
        "v_mfma_f32_4x4x4_16b_bf16 v[84:87], v[96:97], v[66:67], 0\n"
        "s_mov_b64 exec, s[70:71]\n"
        "v_pk_mul_f32 v[72:73], v[72:73], v[98:99] op_sel_hi:[1,0]\n"
        "v_pk_mul_f32 v[74:75], v[74:75], v[98:99] op_sel_hi:[1,0]\n"
        "v_pk_fma_f32 v[72:73], v[76:77], v[98:99], v[72:73] op_sel:[0,1,0] op_sel_hi:[1,1,1]\n"
        "v_pk_fma_f32 v[74:75], v[78:79], v[98:99], v[74:75] op_sel:[0,1,0] op_sel_hi:[1,1,1]\n"
        "v_pk_fma_f32 v[72:73], v[80:81], v[100:101], v[72:73] op_sel_hi:[1,0,1]\n"
        "v_pk_fma_f32 v[74:75], v[82:83], v[100:101], v[74:75] op_sel_hi:[1,0,1]\n"
        "v_pk_fma_f32 v[72:73], v[84:85], v[72:73], s[48:49]\n"
        "v_pk_fma_f32 v[74:75], v[86:87], v[74:75], s[50:51]\n"
        "ds_add_u64 v109, v[72:73] offset:22072\n"
        "ds_add_u64 v109, v[74:75] offset:32072\n"
        "s_mov_b64 exec, s[88:89]\n"
        "s_waitcnt lgkmcnt(4)\n"
        "v_cmp_le_u32_e32 vcc, s56, v112\n"
        "s_cbranch_vccz .Lg38_%=\n"
        "s_mov_b64 exec, vcc\n"
        "ds_add_rtn_u32 v112, v115, v114\n"
        "s_mov_b64 exec, s[88:89]\n"
        "s_waitcnt lgkmcnt(0)\n"
        ".Lg38_%=:\n"
        "v_and_b32 v112, 0x7ffff, v112\n"
        "ds_write_b64 v112, v[34:35]\n"
        "s_mov_b64 exec, s[90:91]\n"
        "s_waitcnt lgkmcnt(4)\n"
        "v_cmp_le_u32_e32 vcc, s56, v113\n"
        "s_cbranch_vccz .Lg39_%=\n"
        "s_mov_b64 exec, vcc\n"
        "ds_add_rtn_u32 v113, v115, v114\n"
        "s_mov_b64 exec, s[90:91]\n"
        "s_waitcnt lgkmcnt(0)\n"
        ".Lg39_%=:\n"
        "v_and_b32 v113, 0x7ffff, v113\n"
        "ds_write_b64 v113, v[36:37]\n"
        "s_mov_b64 exec, -1\n"
        "s_bitcmp1_b32 %[big], 3\n"
        "s_cbranch_scc0 .Lg40_%=\n"
        "s_waitcnt vmcnt(0) lgkmcnt(0)\n"
        "ds_read_b32 v111, v11 offset:624\n"
        "s_waitcnt lgkmcnt(0)\n"
        "v_readlane_b32 s54, v111, 12\n"
        "v_readlane_b32 s55, v111, 13\n"
        "s_nop 0\n"
        "v_mbcnt_lo_u32_b32 v110, s54, 0\n"
        "v_mbcnt_hi_u32_b32 v110, s55, v110\n"
        "s_bcnt1_i32_b64 s54, s[54:55]\n"
        "s_sub_u32 s54, 32, s54\n"
        "s_lshl2_add_u32 s52, s54, s53\n"
        "v_lshl_add_u32 v110, v110, 2, s52\n"
        "ds_read_b32 v110, v110 offset:688\n"
        "s_waitcnt lgkmcnt(0)\n"
        "v_add_u32 v110, v6, v110\n"
        "v_add_u32 v110, 3840, v110\n"
        "v_and_b32 v17, 2, v110\n"
        "v_and_b32 v110, -4, v110\n"
        "buffer_load_dwordx3 v[14:16], v110, s[36:39], 0 offen sc1\n"
        "v_cmp_ne_u32_e32 vcc, 0, v17\n"
        "s_waitcnt vmcnt(0)\n"
        "v_and_b32 v110, 0x7ffc0000, v15\n"
        "v_cmp_class_f32_e64 s[88:89], v15, %[cls]\n"
        "v_cndmask_b32_e32 v17, v14, v16, vcc\n"
        "v_mul_hi_u32 v110, v110, s47\n"
        "v_cndmask_b32_sdwa v16, v16, v14, vcc dst_sel:DWORD dst_unused:UNUSED_PAD src0_sel:WORD_0 src1_sel:WORD_1\n"
        "v_and_b32 v111, 0x7ffc0000, v17\n"
        "v_cmp_class_f32_e64 s[90:91], v17, %[cls]\n"
        "v_mul_hi_u32 v111, v111, s47\n"
        "v_and_b32 v110, -4, v110\n"
        "v_and_b32 v111, -4, v111\n"
        "s_mov_b64 exec, s[88:89]\n"
        "ds_add_rtn_u32 v112, v110, v114 offset:20016\n"
        "s_mov_b64 exec, s[90:91]\n"
        "ds_add_rtn_u32 v113, v111, v114 offset:20016\n"
        "s_mov_b64 exec, -1\n"
        "v_perm_b32 v14, v15, v16, s92\n"
        "v_perm_b32 v16, v17, v16, s93\n"
        "v_and_b32 v14, 0x7ff0000, v14\n"
        "v_and_b32 v16, 0x7ff0000, v16\n"
        "s_mov_b64 exec, s[88:89]\n"
        "s_waitcnt lgkmcnt(1)\n"
        "v_cmpx_le_u32_e32 vcc, s56, v112\n"
        "ds_add_rtn_u32 v112, v115, v114\n"
        "s_mov_b64 exec, s[90:91]\n"
        "s_waitcnt lgkmcnt(1)\n"
        "v_cmpx_le_u32_e32 vcc, s56, v113\n"
        "ds_add_rtn_u32 v113, v115, v114\n"
        "s_mov_b64 exec, -1\n"
        "s_nop 1\n"
        "s_mov_b64 exec, s[88:89]\n"
        "s_waitcnt lgkmcnt(1)\n"
        "v_and_b32 v112, 0x7ffff, v112\n"
        "ds_write_b64 v112, v[14:15]\n"
        "s_mov_b64 exec, s[90:91]\n"
        "s_waitcnt lgkmcnt(1)\n"
        "v_and_b32 v113, 0x7ffff, v113\n"
        "ds_write_b64 v113, v[16:17]\n"
        "s_mov_b64 exec, -1\n"
        "s_waitcnt lgkmcnt(0)\n"
        ".Lg40_%=:\n"
        "s_nop 1\n"
        "s_waitcnt lgkmcnt(0)\n"
        "s_barrier\n"
        "s_setprio 3\n"
        "s_add_u32 s40, s40, 640000\n"
        "s_addc_u32 s41, s41, 0\n"
        "s_sub_u32 s42, s42, 80000\n"
        "ds_read_b32 v110, v8\n"
        "ds_read_b64 v[54:55], v7 offset:0\n"
        "ds_read_b64 v[56:57], v7 offset:520\n"
        "ds_read_b64 v[58:59], v7 offset:1040\n"
        "ds_read_b64 v[60:61], v7 offset:1560\n"
        "ds_read_b64 v[62:63], v7 offset:2080\n"
        "ds_read_b64 v[64:65], v7 offset:2600\n"
        "ds_read_b64 v[38:39], v7 offset:3120\n"
        "ds_read_b64 v[40:41], v7 offset:3640\n"
        "ds_read_b64 v[42:43], v7 offset:4160\n"
        "v_subrev_u32 v111, %[ovd], v7\n"
        "ds_read_b64 v[44:45], v111 offset:4680\n"
        "v_subrev_u32 v111, %[ovd], v111\n"
        "ds_read_b64 v[46:47], v111 offset:5200\n"
        "v_subrev_u32 v111, %[ovd], v111\n"
        "ds_read_b64 v[48:49], v111 offset:5720\n"
        "ds_write_b32 v8, v9\n"
        "s_waitcnt lgkmcnt(13)\n"
        "v_and_b32 v110, 0xffffff, v110\n"
        "v_sub_u32 v110, v110, v10\n"
        "v_ashrrev_i32 v110, 3, v110\n"
        "v_add_u32 v12, v12, v110\n"
        "v_max_i32 v13, v13, v110\n"
        "v_med3_i32 v110, v110, 0, 63\n"
        "s_nop 0\n"
        "v_readlane_b32 s76, v110, 0\n"
        "v_readlane_b32 s78, v110, 1\n"
        "v_readlane_b32 s80, v110, 2\n"
        "v_readlane_b32 s82, v110, 3\n"
        "v_readlane_b32 s84, v110, 4\n"
        "v_readlane_b32 s86, v110, 5\n"
        "v_readlane_b32 s60, v110, 6\n"
        "v_readlane_b32 s62, v110, 7\n"
        "v_readlane_b32 s64, v110, 8\n"
        "v_readlane_b32 s66, v110, 9\n"
        "v_readlane_b32 s68, v110, 10\n"
        "v_readlane_b32 s70, v110, 11\n"
        "s_bfm_b64 s[76:77], s76, 0\n"
        "s_bfm_b64 s[78:79], s78, 0\n"
        "s_bfm_b64 s[80:81], s80, 0\n"
        "s_bfm_b64 s[82:83], s82, 0\n"
        "s_bfm_b64 s[84:85], s84, 0\n"
        "s_bfm_b64 s[86:87], s86, 0\n"
        "s_bfm_b64 s[60:61], s60, 0\n"
        "s_bfm_b64 s[62:63], s62, 0\n"
        "s_bfm_b64 s[64:65], s64, 0\n"
        "s_bfm_b64 s[66:67], s66, 0\n"
        "s_bfm_b64 s[68:69], s68, 0\n"
        "s_bfm_b64 s[70:71], s70, 0\n"
        "s_waitcnt lgkmcnt(12)\n"
        "v_bfe_u32 v101, v55, 14, 17\n"
        "v_pk_mul_f32 v[98:99], v[50:51], s[44:45] op_sel_hi:[0,1]\n"
        "v_mul_f32 v100, s46, v50\n"
        "v_mfma_f32_32x32x16_bf16 v[72:87], v[92:95], v[88:91], 0\n"
        "v_cos_f32 v98, v98\n"
        "v_cos_f32 v99, v99\n"
        "v_cos_f32 v100, v100\n"
        "s_mov_b64 exec, s[74:75]\n"
        "v_and_b32 v109, 0x3ff8, v53\n"
        "v_add_u32 v111, v109, v109\n"
        "ds_read_b128 v[88:91], v111 offset:0\n"
        "s_mov_b64 exec, s[76:77]\n"
        "buffer_load_dwordx2 v[66:67], v101, s[40:43], 0 idxen\n"
        "s_mov_b64 exec, -1\n"
        "s_waitcnt vmcnt(2)\n"
        "s_nop 0\n"
        "v_mfma_f32_4x4x4_16b_bf16 v[84:87], v[96:97], v[68:69], 0\n"
        "s_mov_b64 exec, s[72:73]\n"
        "v_pk_mul_f32 v[72:73], v[72:73], v[98:99] op_sel_hi:[1,0]\n"
        "v_pk_mul_f32 v[74:75], v[74:75], v[98:99] op_sel_hi:[1,0]\n"
        "v_pk_fma_f32 v[72:73], v[76:77], v[98:99], v[72:73] op_sel:[0,1,0] op_sel_hi:[1,1,1]\n"
        "v_pk_fma_f32 v[74:75], v[78:79], v[98:99], v[74:75] op_sel:[0,1,0] op_sel_hi:[1,1,1]\n"
        "v_pk_fma_f32 v[72:73], v[80:81], v[100:101], v[72:73] op_sel_hi:[1,0,1]\n"
        "v_pk_fma_f32 v[74:75], v[82:83], v[100:101], v[74:75] op_sel_hi:[1,0,1]\n"
        "v_pk_fma_f32 v[72:73], v[84:85], v[72:73], s[48:49]\n"
        "v_pk_fma_f32 v[74:75], v[86:87], v[74:75], s[50:51]\n"
        "ds_add_u64 v108, v[72:73] offset:22072\n"
        "ds_add_u64 v108, v[74:75] offset:32072\n"
        "s_mov_b64 exec, -1\n"
        "s_waitcnt lgkmcnt(14)\n"
        "v_bfe_u32 v101, v57, 14, 17\n"
        "v_pk_mul_f32 v[98:99], v[52:53], s[44:45] op_sel_hi:[0,1]\n"
        "v_mul_f32 v100, s46, v52\n"
        "s_waitcnt lgkmcnt(2)\n"
        "v_mfma_f32_32x32x16_bf16 v[72:87], v[92:95], v[88:91], 0\n"
        "v_cos_f32 v98, v98\n"
        "v_cos_f32 v99, v99\n"
        "v_cos_f32 v100, v100\n"
        "s_mov_b64 exec, s[76:77]\n"
        "v_and_b32 v108, 0x3ff8, v55\n"
        "v_add_u32 v111, v108, v108\n"
        "ds_read_b128 v[88:91], v111 offset:0\n"
        "s_mov_b64 exec, s[78:79]\n"
        "buffer_load_dwordx2 v[68:69], v101, s[40:43], 0 idxen\n"
        "s_mov_b64 exec, -1\n"
        "s_waitcnt vmcnt(2)\n"
        "s_nop 0\n"
        "v_mfma_f32_4x4x4_16b_bf16 v[84:87], v[96:97], v[70:71], 0\n"
        "s_mov_b64 exec, s[74:75]\n"
        "v_pk_mul_f32 v[72:73], v[72:73], v[98:99] op_sel_hi:[1,0]\n"
        "v_pk_mul_f32 v[74:75], v[74:75], v[98:99] op_sel_hi:[1,0]\n"
        "v_pk_fma_f32 v[72:73], v[76:77], v[98:99], v[72:73] op_sel:[0,1,0] op_sel_hi:[1,1,1]\n"
        "v_pk_fma_f32 v[74:75], v[78:79], v[98:99], v[74:75] op_sel:[0,1,0] op_sel_hi:[1,1,1]\n"
        "v_pk_fma_f32 v[72:73], v[80:81], v[100:101], v[72:73] op_sel_hi:[1,0,1]\n"
        "v_pk_fma_f32 v[74:75], v[82:83], v[100:101], v[74:75] op_sel_hi:[1,0,1]\n"
        "v_pk_fma_f32 v[72:73], v[84:85], v[72:73], s[48:49]\n"
        "v_pk_fma_f32 v[74:75], v[86:87], v[74:75], s[50:51]\n"
        "ds_add_u64 v109, v[72:73] offset:22072\n"
        "ds_add_u64 v109, v[74:75] offset:32072\n"
        "s_mov_b64 exec, -1\n"
        "v_bfe_u32 v101, v59, 14, 17\n"
        "v_pk_mul_f32 v[98:99], v[54:55], s[44:45] op_sel_hi:[0,1]\n"
        "v_mul_f32 v100, s46, v54\n"
        "s_waitcnt lgkmcnt(2)\n"
        "v_mfma_f32_32x32x16_bf16 v[72:87], v[92:95], v[88:91], 0\n"
        "v_cos_f32 v98, v98\n"
        "v_cos_f32 v99, v99\n"
        "v_cos_f32 v100, v100\n"
        "s_mov_b64 exec, s[78:79]\n"
        "v_and_b32 v109, 0x3ff8, v57\n"
        "v_add_u32 v111, v109, v109\n"
        "ds_read_b128 v[88:91], v111 offset:0\n"
        "s_mov_b64 exec, s[80:81]\n"
        "buffer_load_dwordx2 v[70:71], v101, s[40:43], 0 idxen\n"
        "s_mov_b64 exec, -1\n"
        "s_waitcnt vmcnt(2)\n"
        "s_nop 0\n"
        "v_mfma_f32_4x4x4_16b_bf16 v[84:87], v[96:97], v[66:67], 0\n"
        "s_mov_b64 exec, s[76:77]\n"
        "v_pk_mul_f32 v[72:73], v[72:73], v[98:99] op_sel_hi:[1,0]\n"
        "v_pk_mul_f32 v[74:75], v[74:75], v[98:99] op_sel_hi:[1,0]\n"
        "v_pk_fma_f32 v[72:73], v[76:77], v[98:99], v[72:73] op_sel:[0,1,0] op_sel_hi:[1,1,1]\n"
        "v_pk_fma_f32 v[74:75], v[78:79], v[98:99], v[74:75] op_sel:[0,1,0] op_sel_hi:[1,1,1]\n"
        "v_pk_fma_f32 v[72:73], v[80:81], v[100:101], v[72:73] op_sel_hi:[1,0,1]\n"
        "v_pk_fma_f32 v[74:75], v[82:83], v[100:101], v[74:75] op_sel_hi:[1,0,1]\n"
        "v_pk_fma_f32 v[72:73], v[84:85], v[72:73], s[48:49]\n"
        "v_pk_fma_f32 v[74:75], v[86:87], v[74:75], s[50:51]\n"
        "ds_add_u64 v108, v[72:73] offset:22072\n"
        "ds_add_u64 v108, v[74:75] offset:32072\n"
        "s_mov_b64 exec, -1\n"
        "s_setprio 2\n"
        "v_bfe_u32 v101, v61, 14, 17\n"
        "v_pk_mul_f32 v[98:99], v[56:57], s[44:45] op_sel_hi:[0,1]\n"
        "v_mul_f32 v100, s46, v56\n"
        "s_waitcnt lgkmcnt(2)\n"
        "v_mfma_f32_32x32x16_bf16 v[72:87], v[92:95], v[88:91], 0\n"
        "v_cos_f32 v98, v98\n"
        "v_cos_f32 v99, v99\n"
        "v_cos_f32 v100, v100\n"
        "s_mov_b64 exec, s[80:81]\n"
        "v_and_b32 v108, 0x3ff8, v59\n"
        "v_add_u32 v111, v108, v108\n"
        "ds_read_b128 v[88:91], v111 offset:0\n"
        "s_mov_b64 exec, s[82:83]\n"
        "buffer_load_dwordx2 v[66:67], v101, s[40:43], 0 idxen\n"
        "s_mov_b64 exec, -1\n"
        "s_waitcnt vmcnt(2)\n"
        "s_nop 0\n"
        "v_mfma_f32_4x4x4_16b_bf16 v[84:87], v[96:97], v[68:69], 0\n"
        "s_mov_b64 exec, s[78:79]\n"
        "v_pk_mul_f32 v[72:73], v[72:73], v[98:99] op_sel_hi:[1,0]\n"
        "v_pk_mul_f32 v[74:75], v[74:75], v[98:99] op_sel_hi:[1,0]\n"
        "v_pk_fma_f32 v[72:73], v[76:77], v[98:99], v[72:73] op_sel:[0,1,0] op_sel_hi:[1,1,1]\n"
        "v_pk_fma_f32 v[74:75], v[78:79], v[98:99], v[74:75] op_sel:[0,1,0] op_sel_hi:[1,1,1]\n"
        "v_pk_fma_f32 v[72:73], v[80:81], v[100:101], v[72:73] op_sel_hi:[1,0,1]\n"
        "v_pk_fma_f32 v[74:75], v[82:83], v[100:101], v[74:75] op_sel_hi:[1,0,1]\n"
        "v_pk_fma_f32 v[72:73], v[84:85], v[72:73], s[48:49]\n"
        "v_pk_fma_f32 v[74:75], v[86:87], v[74:75], s[50:51]\n"
        "ds_add_u64 v109, v[72:73] offset:22072\n"
        "ds_add_u64 v109, v[74:75] offset:32072\n"
        "s_mov_b64 exec, -1\n"
        "v_bfe_u32 v101, v63, 14, 17\n"
        "v_pk_mul_f32 v[98:99], v[58:59], s[44:45] op_sel_hi:[0,1]\n"
        "v_mul_f32 v100, s46, v58\n"
        "s_waitcnt lgkmcnt(2)\n"
        "v_mfma_f32_32x32x16_bf16 v[72:87], v[92:95], v[88:91], 0\n"
        "v_cos_f32 v98, v98\n"
        "v_cos_f32 v99, v99\n"
        "v_cos_f32 v100, v100\n"
        "s_mov_b64 exec, s[82:83]\n"
        "v_and_b32 v109, 0x3ff8, v61\n"
        "v_add_u32 v111, v109, v109\n"
        "ds_read_b128 v[88:91], v111 offset:0\n"
        "s_mov_b64 exec, s[84:85]\n"
        "buffer_load_dwordx2 v[68:69], v101, s[40:43], 0 idxen\n"
        "s_mov_b64 exec, -1\n"
        "s_waitcnt vmcnt(2)\n"
        "s_nop 0\n"
        "v_mfma_f32_4x4x4_16b_bf16 v[84:87], v[96:97], v[70:71], 0\n"
        "s_mov_b64 exec, s[80:81]\n"
        "v_pk_mul_f32 v[72:73], v[72:73], v[98:99] op_sel_hi:[1,0]\n"
        "v_pk_mul_f32 v[74:75], v[74:75], v[98:99] op_sel_hi:[1,0]\n"
        "v_pk_fma_f32 v[72:73], v[76:77], v[98:99], v[72:73] op_sel:[0,1,0] op_sel_hi:[1,1,1]\n"
        "v_pk_fma_f32 v[74:75], v[78:79], v[98:99], v[74:75] op_sel:[0,1,0] op_sel_hi:[1,1,1]\n"
        "v_pk_fma_f32 v[72:73], v[80:81], v[100:101], v[72:73] op_sel_hi:[1,0,1]\n"
        "v_pk_fma_f32 v[74:75], v[82:83], v[100:101], v[74:75] op_sel_hi:[1,0,1]\n"
        "v_pk_fma_f32 v[72:73], v[84:85], v[72:73], s[48:49]\n"
        "v_pk_fma_f32 v[74:75], v[86:87], v[74:75], s[50:51]\n"
        "ds_add_u64 v108, v[72:73] offset:22072\n"
        "ds_add_u64 v108, v[74:75] offset:32072\n"
        "s_mov_b64 exec, -1\n"
        "v_bfe_u32 v101, v65, 14, 17\n"
        "v_pk_mul_f32 v[98:99], v[60:61], s[44:45] op_sel_hi:[0,1]\n"
        "v_mul_f32 v100, s46, v60\n"
        "s_waitcnt lgkmcnt(2)\n"
        "v_mfma_f32_32x32x16_bf16 v[72:87], v[92:95], v[88:91], 0\n"
        "v_cos_f32 v98, v98\n"
        "v_cos_f32 v99, v99\n"
        "v_cos_f32 v100, v100\n"
        "s_mov_b64 exec, s[84:85]\n"
        "v_and_b32 v108, 0x3ff8, v63\n"
        "v_add_u32 v111, v108, v108\n"
        "ds_read_b128 v[88:91], v111 offset:0\n"
        "s_mov_b64 exec, s[86:87]\n"
        "buffer_load_dwordx2 v[70:71], v101, s[40:43], 0 idxen\n"
        "s_mov_b64 exec, -1\n"
        "s_waitcnt vmcnt(2)\n"
        "s_nop 0\n"
        "v_mfma_f32_4x4x4_16b_bf16 v[84:87], v[96:97], v[66:67], 0\n"
        "s_mov_b64 exec, s[82:83]\n"
        "v_pk_mul_f32 v[72:73], v[72:73], v[98:99] op_sel_hi:[1,0]\n"
        "v_pk_mul_f32 v[74:75], v[74:75], v[98:99] op_sel_hi:[1,0]\n"
        "v_pk_fma_f32 v[72:73], v[76:77], v[98:99], v[72:73] op_sel:[0,1,0] op_sel_hi:[1,1,1]\n"
        "v_pk_fma_f32 v[74:75], v[78:79], v[98:99], v[74:75] op_sel:[0,1,0] op_sel_hi:[1,1,1]\n"
        "v_pk_fma_f32 v[72:73], v[80:81], v[100:101], v[72:73] op_sel_hi:[1,0,1]\n"
        "v_pk_fma_f32 v[74:75], v[82:83], v[100:101], v[74:75] op_sel_hi:[1,0,1]\n"
        "v_pk_fma_f32 v[72:73], v[84:85], v[72:73], s[48:49]\n"
        "v_pk_fma_f32 v[74:75], v[86:87], v[74:75], s[50:51]\n"
        "ds_add_u64 v109, v[72:73] offset:22072\n"
        "ds_add_u64 v109, v[74:75] offset:32072\n"
        "s_mov_b64 exec, -1\n"
        "s_setprio 1\n"
        "v_bfe_u32 v101, v39, 14, 17\n"
        "v_pk_mul_f32 v[98:99], v[62:63], s[44:45] op_sel_hi:[0,1]\n"
        "v_mul_f32 v100, s46, v62\n"
        "s_waitcnt lgkmcnt(2)\n"
        "v_mfma_f32_32x32x16_bf16 v[72:87], v[92:95], v[88:91], 0\n"
        "v_cos_f32 v98, v98\n"
        "v_cos_f32 v99, v99\n"
        "v_cos_f32 v100, v100\n"
        "s_mov_b64 exec, s[86:87]\n"
        "v_and_b32 v109, 0x3ff8, v65\n"
        "v_add_u32 v111, v109, v109\n"
        "ds_read_b128 v[88:91], v111 offset:0\n"
        "s_mov_b64 exec, s[60:61]\n"
        "buffer_load_dwordx2 v[66:67], v101, s[40:43], 0 idxen\n"
        "s_mov_b64 exec, -1\n"
        "s_waitcnt vmcnt(2)\n"
        "s_nop 0\n"
        "v_mfma_f32_4x4x4_16b_bf16 v[84:87], v[96:97], v[68:69], 0\n"
        "s_mov_b64 exec, s[84:85]\n"
        "v_pk_mul_f32 v[72:73], v[72:73], v[98:99] op_sel_hi:[1,0]\n"
        "v_pk_mul_f32 v[74:75], v[74:75], v[98:99] op_sel_hi:[1,0]\n"
        "v_pk_fma_f32 v[72:73], v[76:77], v[98:99], v[72:73] op_sel:[0,1,0] op_sel_hi:[1,1,1]\n"
        "v_pk_fma_f32 v[74:75], v[78:79], v[98:99], v[74:75] op_sel:[0,1,0] op_sel_hi:[1,1,1]\n"
        "v_pk_fma_f32 v[72:73], v[80:81], v[100:101], v[72:73] op_sel_hi:[1,0,1]\n"
        "v_pk_fma_f32 v[74:75], v[82:83], v[100:101], v[74:75] op_sel_hi:[1,0,1]\n"
        "v_pk_fma_f32 v[72:73], v[84:85], v[72:73], s[48:49]\n"
        "v_pk_fma_f32 v[74:75], v[86:87], v[74:75], s[50:51]\n"
        "ds_add_u64 v108, v[72:73] offset:22072\n"
        "ds_add_u64 v108, v[74:75] offset:32072\n"
        "s_mov_b64 exec, -1\n"
        "v_bfe_u32 v101, v41, 14, 17\n"
        "v_pk_mul_f32 v[98:99], v[64:65], s[44:45] op_sel_hi:[0,1]\n"
        "v_mul_f32 v100, s46, v64\n"
        "s_waitcnt lgkmcnt(2)\n"
        "v_mfma_f32_32x32x16_bf16 v[72:87], v[92:95], v[88:91], 0\n"
        "v_cos_f32 v98, v98\n"
        "v_cos_f32 v99, v99\n"
        "v_cos_f32 v100, v100\n"
        "s_mov_b64 exec, s[60:61]\n"
        "v_and_b32 v108, 0x3ff8, v39\n"
        "v_add_u32 v111, v108, v108\n"
        "ds_read_b128 v[88:91], v111 offset:0\n"
        "s_mov_b64 exec, s[62:63]\n"
        "buffer_load_dwordx2 v[68:69], v101, s[40:43], 0 idxen\n"
        "s_mov_b64 exec, -1\n"
        "s_waitcnt vmcnt(2)\n"
        "s_nop 0\n"
        "v_mfma_f32_4x4x4_16b_bf16 v[84:87], v[96:97], v[70:71], 0\n"
        "s_mov_b64 exec, s[86:87]\n"
        "v_pk_mul_f32 v[72:73], v[72:73], v[98:99] op_sel_hi:[1,0]\n"
        "v_pk_mul_f32 v[74:75], v[74:75], v[98:99] op_sel_hi:[1,0]\n"
        "v_pk_fma_f32 v[72:73], v[76:77], v[98:99], v[72:73] op_sel:[0,1,0] op_sel_hi:[1,1,1]\n"
        "v_pk_fma_f32 v[74:75], v[78:79], v[98:99], v[74:75] op_sel:[0,1,0] op_sel_hi:[1,1,1]\n"
        "v_pk_fma_f32 v[72:73], v[80:81], v[100:101], v[72:73] op_sel_hi:[1,0,1]\n"
        "v_pk_fma_f32 v[74:75], v[82:83], v[100:101], v[74:75] op_sel_hi:[1,0,1]\n"
        "v_pk_fma_f32 v[72:73], v[84:85], v[72:73], s[48:49]\n"
        "v_pk_fma_f32 v[74:75], v[86:87], v[74:75], s[50:51]\n"
        "ds_add_u64 v109, v[72:73] offset:22072\n"
        "ds_add_u64 v109, v[74:75] offset:32072\n"
        "s_mov_b64 exec, -1\n"
        "v_bfe_u32 v101, v43, 14, 17\n"
        "v_pk_mul_f32 v[98:99], v[38:39], s[44:45] op_sel_hi:[0,1]\n"
        "v_mul_f32 v100, s46, v38\n"
        "s_waitcnt lgkmcnt(2)\n"
        "v_mfma_f32_32x32x16_bf16 v[72:87], v[92:95], v[88:91], 0\n"
        "v_cos_f32 v98, v98\n"
        "v_cos_f32 v99, v99\n"
        "v_cos_f32 v100, v100\n"
        "s_mov_b64 exec, s[62:63]\n"
        "v_and_b32 v109, 0x3ff8, v41\n"
        "v_add_u32 v111, v109, v109\n"
        "ds_read_b128 v[88:91], v111 offset:0\n"
        "s_mov_b64 exec, s[64:65]\n"
        "buffer_load_dwordx2 v[70:71], v101, s[40:43], 0 idxen\n"
        "s_mov_b64 exec, -1\n"
        "s_waitcnt vmcnt(2)\n"
        "s_nop 0\n"
        "v_mfma_f32_4x4x4_16b_bf16 v[84:87], v[96:97], v[66:67], 0\n"
        "s_mov_b64 exec, s[60:61]\n"
        "v_pk_mul_f32 v[72:73], v[72:73], v[98:99] op_sel_hi:[1,0]\n"
        "v_pk_mul_f32 v[74:75], v[74:75], v[98:99] op_sel_hi:[1,0]\n"
        "v_pk_fma_f32 v[72:73], v[76:77], v[98:99], v[72:73] op_sel:[0,1,0] op_sel_hi:[1,1,1]\n"
        "v_pk_fma_f32 v[74:75], v[78:79], v[98:99], v[74:75] op_sel:[0,1,0] op_sel_hi:[1,1,1]\n"
        "v_pk_fma_f32 v[72:73], v[80:81], v[100:101], v[72:73] op_sel_hi:[1,0,1]\n"
        "v_pk_fma_f32 v[74:75], v[82:83], v[100:101], v[74:75] op_sel_hi:[1,0,1]\n"
        "v_pk_fma_f32 v[72:73], v[84:85], v[72:73], s[48:49]\n"
        "v_pk_fma_f32 v[74:75], v[86:87], v[74:75], s[50:51]\n"
        "ds_add_u64 v108, v[72:73] offset:22072\n"
        "ds_add_u64 v108, v[74:75] offset:32072\n"
        "s_mov_b64 exec, -1\n"
        "s_setprio 0\n"
        "v_bfe_u32 v101, v45, 14, 17\n"
        "v_pk_mul_f32 v[98:99], v[40:41], s[44:45] op_sel_hi:[0,1]\n"
        "v_mul_f32 v100, s46, v40\n"
        "s_waitcnt lgkmcnt(2)\n"
        "v_mfma_f32_32x32x16_bf16 v[72:87], v[92:95], v[88:91], 0\n"
        "v_cos_f32 v98, v98\n"
        "v_cos_f32 v99, v99\n"
        "v_cos_f32 v100, v100\n"
        "s_mov_b64 exec, s[64:65]\n"
        "v_and_b32 v108, 0x3ff8, v43\n"
        "v_add_u32 v111, v108, v108\n"
        "ds_read_b128 v[88:91], v111 offset:0\n"
        "s_mov_b64 exec, s[66:67]\n"
        "buffer_load_dwordx2 v[66:67], v101, s[40:43], 0 idxen\n"
        "s_mov_b64 exec, -1\n"
        "s_waitcnt vmcnt(2)\n"
        "s_nop 0\n"
        "v_mfma_f32_4x4x4_16b_bf16 v[84:87], v[96:97], v[68:69], 0\n"
        "s_mov_b64 exec, s[62:63]\n"
        "v_pk_mul_f32 v[72:73], v[72:73], v[98:99] op_sel_hi:[1,0]\n"
        "v_pk_mul_f32 v[74:75], v[74:75], v[98:99] op_sel_hi:[1,0]\n"
        "v_pk_fma_f32 v[72:73], v[76:77], v[98:99], v[72:73] op_sel:[0,1,0] op_sel_hi:[1,1,1]\n"
        "v_pk_fma_f32 v[74:75], v[78:79], v[98:99], v[74:75] op_sel:[0,1,0] op_sel_hi:[1,1,1]\n"
        "v_pk_fma_f32 v[72:73], v[80:81], v[100:101], v[72:73] op_sel_hi:[1,0,1]\n"
        "v_pk_fma_f32 v[74:75], v[82:83], v[100:101], v[74:75] op_sel_hi:[1,0,1]\n"
        "v_pk_fma_f32 v[72:73], v[84:85], v[72:73], s[48:49]\n"
        "v_pk_fma_f32 v[74:75], v[86:87], v[74:75], s[50:51]\n"
        "ds_add_u64 v109, v[72:73] offset:22072\n"
        "ds_add_u64 v109, v[74:75] offset:32072\n"
        "s_mov_b64 exec, -1\n"
        "v_bfe_u32 v101, v47, 14, 17\n"
        "v_pk_mul_f32 v[98:99], v[42:43], s[44:45] op_sel_hi:[0,1]\n"
        "v_mul_f32 v100, s46, v42\n"
        "s_waitcnt lgkmcnt(2)\n"
        "v_mfma_f32_32x32x16_bf16 v[72:87], v[92:95], v[88:91], 0\n"
        "v_cos_f32 v98, v98\n"
        "v_cos_f32 v99, v99\n"
        "v_cos_f32 v100, v100\n"
        "s_mov_b64 exec, s[66:67]\n"
        "v_and_b32 v109, 0x3ff8, v45\n"
        "v_add_u32 v111, v109, v109\n"
        "ds_read_b128 v[88:91], v111 offset:0\n"
        "s_mov_b64 exec, s[68:69]\n"
        "buffer_load_dwordx2 v[68:69], v101, s[40:43], 0 idxen\n"
        "s_mov_b64 exec, -1\n"
        "s_waitcnt vmcnt(2)\n"
        "s_nop 0\n"
        "v_mfma_f32_4x4x4_16b_bf16 v[84:87], v[96:97], v[70:71], 0\n"
        "s_mov_b64 exec, s[64:65]\n"
        "v_pk_mul_f32 v[72:73], v[72:73], v[98:99] op_sel_hi:[1,0]\n"
        "v_pk_mul_f32 v[74:75], v[74:75], v[98:99] op_sel_hi:[1,0]\n"
        "v_pk_fma_f32 v[72:73], v[76:77], v[98:99], v[72:73] op_sel:[0,1,0] op_sel_hi:[1,1,1]\n"
        "v_pk_fma_f32 v[74:75], v[78:79], v[98:99], v[74:75] op_sel:[0,1,0] op_sel_hi:[1,1,1]\n"
        "v_pk_fma_f32 v[72:73], v[80:81], v[100:101], v[72:73] op_sel_hi:[1,0,1]\n"
        "v_pk_fma_f32 v[74:75], v[82:83], v[100:101], v[74:75] op_sel_hi:[1,0,1]\n"
        "v_pk_fma_f32 v[72:73], v[84:85], v[72:73], s[48:49]\n"
        "v_pk_fma_f32 v[74:75], v[86:87], v[74:75], s[50:51]\n"
        "ds_add_u64 v108, v[72:73] offset:22072\n"
        "ds_add_u64 v108, v[74:75] offset:32072\n"
        "s_mov_b64 exec, -1\n"
        "v_bfe_u32 v101, v49, 14, 17\n"
        "v_pk_mul_f32 v[98:99], v[44:45], s[44:45] op_sel_hi:[0,1]\n"
        "v_mul_f32 v100, s46, v44\n"
        "s_waitcnt lgkmcnt(2)\n"
        "v_mfma_f32_32x32x16_bf16 v[72:87], v[92:95], v[88:91], 0\n"
        "v_cos_f32 v98, v98\n"
        "v_cos_f32 v99, v99\n"
        "v_cos_f32 v100, v100\n"
        "s_mov_b64 exec, s[68:69]\n"
        "v_and_b32 v108, 0x3ff8, v47\n"
        "v_add_u32 v111, v108, v108\n"
        "ds_read_b128 v[88:91], v111 offset:0\n"
        "s_mov_b64 exec, s[70:71]\n"
        "buffer_load_dwordx2 v[70:71], v101, s[40:43], 0 idxen\n"
        "s_mov_b64 exec, -1\n"
        "s_waitcnt vmcnt(2)\n"
        "s_nop 0\n"
        "v_mfma_f32_4x4x4_16b_bf16 v[84:87], v[96:97], v[66:67], 0\n"
        "s_mov_b64 exec, s[66:67]\n"
        "v_pk_mul_f32 v[72:73], v[72:73], v[98:99] op_sel_hi:[1,0]\n"
        "v_pk_mul_f32 v[74:75], v[74:75], v[98:99] op_sel_hi:[1,0]\n"
        "v_pk_fma_f32 v[72:73], v[76:77], v[98:99], v[72:73] op_sel:[0,1,0] op_sel_hi:[1,1,1]\n"
        "v_pk_fma_f32 v[74:75], v[78:79], v[98:99], v[74:75] op_sel:[0,1,0] op_sel_hi:[1,1,1]\n"
        "v_pk_fma_f32 v[72:73], v[80:81], v[100:101], v[72:73] op_sel_hi:[1,0,1]\n"
        "v_pk_fma_f32 v[74:75], v[82:83], v[100:101], v[74:75] op_sel_hi:[1,0,1]\n"
        "v_pk_fma_f32 v[72:73], v[84:85], v[72:73], s[48:49]\n"
        "v_pk_fma_f32 v[74:75], v[86:87], v[74:75], s[50:51]\n"
        "ds_add_u64 v109, v[72:73] offset:22072\n"
        "ds_add_u64 v109, v[74:75] offset:32072\n"
        "s_mov_b64 exec, -1\n"
        "v_min_u32 v102, 624, %[tid]\n"
        "v_lshlrev_b32 v102, 4, v102\n"
        "global_load_dwordx2 v[14:15], v102, %[po] offset:0\n"
        "global_load_dwordx2 v[18:19], v102, %[po] offset:8\n"
        "v_pk_mul_f32 v[98:99], v[46:47], s[44:45] op_sel_hi:[0,1]\n"
        "v_mul_f32 v100, s46, v46\n"
        "s_waitcnt lgkmcnt(2)\n"
        "v_mfma_f32_32x32x16_bf16 v[72:87], v[92:95], v[88:91], 0\n"
        "v_cos_f32 v98, v98\n"
        "v_cos_f32 v99, v99\n"
        "v_cos_f32 v100, v100\n"
        "s_mov_b64 exec, s[70:71]\n"
        "v_and_b32 v109, 0x3ff8, v49\n"
        "v_add_u32 v111, v109, v109\n"
        "ds_read_b128 v[88:91], v111 offset:0\n"
        "s_mov_b64 exec, -1\n"
        "s_waitcnt vmcnt(3)\n"
        "s_nop 2\n"
        "v_mfma_f32_4x4x4_16b_bf16 v[84:87], v[96:97], v[68:69], 0\n"
        "s_mov_b64 exec, s[68:69]\n"
        "v_pk_mul_f32 v[72:73], v[72:73], v[98:99] op_sel_hi:[1,0]\n"
        "v_pk_mul_f32 v[74:75], v[74:75], v[98:99] op_sel_hi:[1,0]\n"
        "v_pk_fma_f32 v[72:73], v[76:77], v[98:99], v[72:73] op_sel:[0,1,0] op_sel_hi:[1,1,1]\n"
        "v_pk_fma_f32 v[74:75], v[78:79], v[98:99], v[74:75] op_sel:[0,1,0] op_sel_hi:[1,1,1]\n"
        "v_pk_fma_f32 v[72:73], v[80:81], v[100:101], v[72:73] op_sel_hi:[1,0,1]\n"
        "v_pk_fma_f32 v[74:75], v[82:83], v[100:101], v[74:75] op_sel_hi:[1,0,1]\n"
        "v_pk_fma_f32 v[72:73], v[84:85], v[72:73], s[48:49]\n"
        "v_pk_fma_f32 v[74:75], v[86:87], v[74:75], s[50:51]\n"
        "ds_add_u64 v108, v[72:73] offset:22072\n"
        "ds_add_u64 v108, v[74:75] offset:32072\n"
        "s_mov_b64 exec, -1\n"
        "v_pk_mul_f32 v[98:99], v[48:49], s[44:45] op_sel_hi:[0,1]\n"
        "v_mul_f32 v100, s46, v48\n"
        "s_waitcnt lgkmcnt(2)\n"
        "v_mfma_f32_32x32x16_bf16 v[72:87], v[92:95], v[88:91], 0\n"
        "v_cos_f32 v98, v98\n"
        "v_cos_f32 v99, v99\n"
        "v_cos_f32 v100, v100\n"
        "s_waitcnt vmcnt(2)\n"
        "s_nop 7\n"
        "v_mfma_f32_4x4x4_16b_bf16 v[84:87], v[96:97], v[70:71], 0\n"
        "s_mov_b64 exec, s[70:71]\n"
        "v_pk_mul_f32 v[72:73], v[72:73], v[98:99] op_sel_hi:[1,0]\n"
        "v_pk_mul_f32 v[74:75], v[74:75], v[98:99] op_sel_hi:[1,0]\n"
        "v_pk_fma_f32 v[72:73], v[76:77], v[98:99], v[72:73] op_sel:[0,1,0] op_sel_hi:[1,1,1]\n"
        "v_pk_fma_f32 v[74:75], v[78:79], v[98:99], v[74:75] op_sel:[0,1,0] op_sel_hi:[1,1,1]\n"
        "v_pk_fma_f32 v[72:73], v[80:81], v[100:101], v[72:73] op_sel_hi:[1,0,1]\n"
        "v_pk_fma_f32 v[74:75], v[82:83], v[100:101], v[74:75] op_sel_hi:[1,0,1]\n"
        "v_pk_fma_f32 v[72:73], v[84:85], v[72:73], s[48:49]\n"
        "v_pk_fma_f32 v[74:75], v[86:87], v[74:75], s[50:51]\n"
        "ds_add_u64 v109, v[72:73] offset:22072\n"
        "ds_add_u64 v109, v[74:75] offset:32072\n"
        "s_mov_b64 exec, -1\n"
        "s_setprio 0\n"
        "s_waitcnt vmcnt(0) lgkmcnt(0)\n"
        "s_nop 4\n"
        :
          "+{v[14:17]}"(raw[0]), "+{v[18:21]}"(raw[1]), "+{v[22:25]}"(raw[2]), "+{v[26:29]}"(raw[3]),
          "+{v[30:33]}"(raw[4]), "+{v[34:37]}"(raw[5]), "+{v12}"(nsum), "+{v13}"(chk), "+{v72}"(f0), "+{v73}"(f1),
          "+{v74}"(f2), [tid] "+v"(tid)
        :
          "{v6}"(lane10), "{v7}"(rbase), "{v8}"(cntr), "{v9}"(cnti), "{v10}"(csub), "{v11}"(mka), [pr] "s"(pr),
          [pm] "s"(pm), [scr] "s"(scr), [po] "s"(po), [nrec] "s"(nrec), [ovd] "s"(ovd), [big] "s"(big), [cls] "s"(cls)
        :
          "v38", "v39", "v40", "v41", "v42", "v43", "v44", "v45", "v46", "v47", "v48", "v49", "v50", "v51", "v52",
          "v53", "v54", "v55", "v56", "v57", "v58", "v59", "v60", "v61", "v62", "v63", "v64", "v65", "v66", "v67",
          "v68", "v69", "v70", "v71", "v75", "v76", "v77", "v78", "v79", "v80", "v81", "v82", "v83", "v84", "v85",
          "v86", "v87", "v88", "v89", "v90", "v91", "v92", "v93", "v94", "v95", "v96", "v97", "v98", "v99", "v100",
          "v101", "v102", "v103", "v104", "v105", "v106", "v107", "v108", "v109", "v110", "v111", "v112", "v113",
          "v114", "v115", "s36", "s37", "s38", "s39", "s40", "s41", "s42", "s43", "s44", "s45", "s46", "s47", "s48",
          "s49", "s50", "s51", "s52", "s53", "s54", "s55", "s56", "s60", "s61", "s62", "s63", "s64", "s65", "s66",
          "s67", "s68", "s69", "s70", "s71", "s72", "s73", "s74", "s75", "s76", "s77", "s78", "s79", "s80", "s81",
          "s82", "s83", "s84", "s85", "s86", "s87", "s88", "s89", "s90", "s91", "s92", "s93", "vcc", "scc", "memory");

        lane = tid & 63;
        mrow = (u32x4){raw[0].x, raw[0].y, raw[1].x, raw[1].y};
        const bool regular = w * S + min(lane, (u32)(S - 1)) < NBIN;
        if (!regular || lane >= S) nsum = 0;
        ok &= chk <= (regular ? 255 : NOVF * 63);
    }

    const u32x4 mx = *lp<u32x4>(L_MX);
    const u32x4 cu = *lp<u32x4>(L_W4 + 64 + (lane & 3) * 16);
    int jd[4];
#pragma unroll
    for (int k = 0; k < 4; ++k) jd[k] = max((int)(mx[k] >> 23), 64) - 126;
    u32x2 wu;
#pragma unroll
    for (int h = 0; h < 2; ++h) {
        const u32 wbits = cu[h], nz = cu[2 + h];
        const u16x2 sh = {(u16)(jd[2 * h] - VBITS), (u16)(jd[2 * h + 1] - VBITS)};
        const u16x2 r = __builtin_bit_cast(u16x2, nz) * sh + __builtin_bit_cast(u16x2, wbits);
        wu[h] = __builtin_bit_cast(u32, r);
    }
    float mf[8], gf[8];
#pragma unroll
    for (int i = 0; i < 4; ++i) {
        mf[2 * i] = lo2f(mrow[i]), mf[2 * i + 1] = hi2f(mrow[i]);
        gf[2 * i] = lo2f(gs[i]), gf[2 * i + 1] = hi2f(gs[i]);
    }
    auto r_out = raw_rsrc(P.out + e0 * 4, OSL * 8);
    if (w < EW) {
        asm volatile("" : "+v"(wu), "+v"(mf[0]), "+v"(mf[1]), "+v"(mf[2]), "+v"(mf[3]), "+v"(mf[4]), "+v"(mf[5]), "+v"(mf[6]), "+v"(mf[7]));
        asm volatile("" : "+v"(gf[0]), "+v"(gf[1]), "+v"(gf[2]), "+v"(gf[3]), "+v"(gf[4]), "+v"(gf[5]), "+v"(gf[6]), "+v"(gf[7]));
    }
    __syncthreads();
    if (w == EW) {
        const u32 k = lane & 3, col = (lane >> 2) & 3, up = (lane >> 4) & 1;
        const u32 wbits = *lp<u16>(L_W4 + up * 64 + col * 16 + k * 2);
        const u32 mv = *lp<u32>(L_MX + (up ? k : col) * 4), ex = mv >> 23;
        const float sm = *lp<float>(L_SM + (up ? k : col) * 4), cs = *lp<float>(L_CS + (up ? k : col) * 4);
        const f32x4 ca = *lp<f32x4>(L_CS);
        const float spread = __uint_as_float(mv) * (float)kE * fmaxf(fmaxf(ca.x, ca.y), fmaxf(ca.z, ca.w));
        const int j = (int)max(ex, 64u) - 126;
        ok &= (ex < 190) & ((mv == 0) | ((ex >= 64) & ((cs == 0.f) | (spread <= 256.f * sm * cs))));
        scale_w(wbits, up ? j - VBITS : (col == 0 ? SH0 : SH1) - j, ok);
    }
    if (w < EW) {
        const u32 ep = min(tid, (u32)(OSL / 2 - 1));
        const u32x4 fa = *lp<u32x4>(L_ACC0 + ep * 16), fb = *lp<u32x4>(L_ACC1 + ep * 16);
        u32x4 res;
        u32 nn = 0;
#pragma unroll
        for (int i = 0; i < 2; ++i) {
            const u32 f0 = fa[2 * i], f1 = fa[2 * i + 1], f2 = fb[2 * i], f3 = fb[2 * i + 1];
            const u32 n0 = (u32)fmaf((float)f0, 1.0f / (float)BM0, 0.5f);
            const u32 n = n0 + (u32)__float2int_rn((float)(int)(f2 - n0 * BM1) * (1.0f / (float)BM1));
            const u32 nb = n * BM1;
            const int v2 = (int)(f2 - nb);
            const u32 carry = (u32)(((long long)(int)n * (long long)BM1 + v2) >> 32);
            const int v0 = (int)(f0 - n * BM0), v1 = (int)(f1 - nb), v3 = (int)(f3 - nb - carry);
            const f32x4 u = mfma4(wu, (u32x2){pk_bf16((float)v0, (float)v1), pk_bf16((float)v2, (float)v3)});
#pragma unroll
            for (int h = 0; h < 2; ++h) {
                const int c = 4 * i + 2 * h;
                res[2 * i + h] = pk_bf16(mf[c] + rbf16(rbf16(u[2 * h]) * gf[c]), mf[c + 1] + rbf16(rbf16(u[2 * h + 1]) * gf[c + 1]));
            }
            nn += n;
            ok &= n <= NCAP;
        }
        __builtin_amdgcn_raw_buffer_store_b128(res, r_out, tid * 16, 0, AUX_SC1);
        if (tid < OSL / 2) nsum -= nn;
    }
    const u32 nw = __builtin_amdgcn_readlane(wave_incl_scan(nsum), 63);
    if (lane == 0) lds_add(L_NSUM, nw);
    if (!ok) *lp<u32>(L_FAIL) = 1;
    __syncthreads();
    const u32x2 fl = *lp<u32x2>(L_FAIL);
    if (__builtin_expect(__builtin_amdgcn_readfirstlane(fl.x | fl.y) != 0, 0)) {
        asm volatile("s_waitcnt vmcnt(0)" ::: "memory");
        __syncthreads();
        accumulate_generic(*fresh_params(), b, tid);
    }
}

__global__ __launch_bounds__(1024, 4) void k_all(Params P) {
    __shared__ __attribute__((aligned(16))) u8 smem[L_TOTAL];
    asm volatile("" :: "v"((LDS u8*)smem) : "memory");
    const u32 tid = threadIdx.x;
    const u32 w = __builtin_amdgcn_readfirstlane(tid >> 6);
    const u32 b = (blockIdx.x & 7) * 32 + (blockIdx.x >> 3);
    const u32x4 gs = partition(P, b, tid, w);
    accumulate(P, b, tid, w, gs);
}

}

extern "C" void kernel_launch(void* const* d_in, const int* in_sizes, int n_in,
                              void* d_out, int out_size, void* d_ws, size_t ws_size,
                              hipStream_t stream) {
    Params P;
    P.m = (const u16*)d_in[0];
    P.idx_in = (const u32*)d_in[1];
    P.idx_out = (const u32*)d_in[2];
    P.dist = (const u16*)d_in[3];
    P.angle = (const u16*)d_in[4];
    P.centers = (const u16*)d_in[5];
    P.freq = (const u16*)d_in[6];
    P.w_rbf = (const u16*)d_in[7];
    P.w_sbf = (const u16*)d_in[8];
    P.w_down = (const u16*)d_in[9];
    P.w_up = (const u16*)d_in[10];
    P.rec = (u8*)d_ws;
    P.flags = (u32*)(P.rec + WS_FLAGS);
    P.out = (u16*)d_out;
    k_all<<<NWG, 1024, 0, stream>>>(P);
}
```
